```python
import torch

def _softmax(x, axis=-1):
    return torch.softmax(x, dim=axis)
def _gelu(x):
    return torch.nn.functional.gelu(x, approximate="tanh")
def _silu(x):
    return torch.nn.functional.silu(x)
def _sigmoid(x):
    return torch.sigmoid(x)
def _layernorm(x, axis=-1, eps=1e-5):
    m = x.mean(dim=axis, keepdim=True)
    v = x.var(dim=axis, keepdim=True, unbiased=False)
    return (x - m) / torch.sqrt(v + eps)
def _scatter_add(tgt, idx, src, axis=0):
    return tgt.clone().index_add_(axis, idx, src)
def _segment_sum(src, seg, n):
    out = torch.zeros((n,) + src.shape[1:], dtype=src.dtype)
    return out.index_add_(0, seg, src)
def _pad(x, pw):
    flat = tuple(v for pair in reversed(pw) for v in pair)
    return torch.nn.functional.pad(x, flat)

kernel_name = 'interleave_layers__spectral_gfnet__dense_activation_gelu__prompt'

def setup_inputs():

    _w = (lambda a: (lambda t: t.to(torch.bfloat16) if t.is_floating_point() else t)(torch.as_tensor(a)))
    def _A_setup():
        B, S, D = 1, 2048, 640
        Df = D // 2 + 1
        return {
            "x": _w((lambda *s: torch.randn(*s, dtype=torch.bfloat16) * 0.05)(B, S, D)),
            "w_r": _w((lambda *s: torch.randn(*s, dtype=torch.bfloat16) * 0.4)(S, Df) * 0.02),
            "w_i": _w((lambda *s: torch.randn(*s, dtype=torch.bfloat16) * 0.4)(S, Df) * 0.02),
        }
    _A_in = _A_setup()

    def _B_setup():
        B, S, D, F = 1, 2048, 640, 1728
        return {
            "x": _w((lambda *s: torch.randn(*s, dtype=torch.bfloat16) * 0.05)(B, S, D)),
            "w1": _w((lambda *s: torch.randn(*s, dtype=torch.bfloat16) * 0.4)(D, F) * (D ** -0.5)),
            "b1": _w((lambda *s: torch.randn(*s, dtype=torch.bfloat16) * 12.8)(F) * 0.01),
            "w2": _w((lambda *s: torch.randn(*s, dtype=torch.bfloat16) * 0.1)(F, D) * (F ** -0.5)),
            "b2": _w((lambda *s: torch.randn(*s, dtype=torch.bfloat16) * 0.8)(D) * 0.01),
        }
    _B_in = _B_setup()

    return {
        "A_x": _A_in["x"],
        "A_w_r": _A_in["w_r"],
        "A_w_i": _A_in["w_i"],
        "B_x": _B_in["x"],
        "B_w1": _B_in["w1"],
        "B_b1": _B_in["b1"],
        "B_w2": _B_in["w2"],
        "B_b2": _B_in["b2"],
    }

def reference(A_x, A_w_r, A_w_i, B_x, B_w1, B_b1, B_w2, B_b2):
    def _A_ref(x, w_r, w_i):
        B, S, D = x.shape
        X = (lambda x, axes=(-2,-1): torch.fft.rfft2(x.float(), dim=axes))(x, axes=(-2, -1))
        W = w_r + 1j * w_i
        X = X * W
        y = (lambda x, s, axes=(-2,-1): torch.fft.irfft2(x, s=s, dim=axes).to(torch.bfloat16))(X, (S, D), axes=(-2, -1))
        return y

    def _B_ref(x, w1, b1, w2, b2):
        h = torch.einsum("bsd,df->bsf", x, w1) + b1
        h = _gelu(h)
        return torch.einsum("bsf,fd->bsd", h, w2) + b2

    x = A_x
    x = x + _A_ref(_layernorm(x, axis=-1), A_w_r, A_w_i)
    x = x + _B_ref(_layernorm(x, axis=-1), B_w1, B_b1, B_w2, B_b2)
    x = x + _A_ref(_layernorm(x, axis=-1), A_w_r, A_w_i)
    x = x + _B_ref(_layernorm(x, axis=-1), B_w1, B_b1, B_w2, B_b2)
    return x
```

```cpp
#include <hip/hip_runtime.h>
#include <hip/hip_bf16.h>
#include <stdint.h>

typedef __attribute__((ext_vector_type(8))) short bf16x8;
typedef __attribute__((ext_vector_type(4))) float f32x4;
typedef __attribute__((ext_vector_type(2))) float cf;
typedef __attribute__((ext_vector_type(4))) unsigned int u32x4;
typedef __attribute__((ext_vector_type(2))) unsigned int u32x2;

constexpr int S = 2048, D = 640, DF = 321, FF = 1728;

struct __attribute__((aligned(8))) cfs { float x, y; };
__device__ const cfs TW640_[640] = {
{0x1.0000000000000p+0f,0x0.0p+0f},{0x1.fff9ae0000000p-1f,-0x1.41b1a40000000p-7f},{0x1.ffe6bc0000000p-1f,-0x1.41adac0000000p-6f},{0x1.ffc7280000000p-1f,-0x1.e27a980000000p-6f},
{0x1.ff9af20000000p-1f,-0x1.419dce0000000p-5f},{0x1.ff621e0000000p-1f,-0x1.91f6600000000p-5f},{0x1.ff1cac0000000p-1f,-0x1.e245060000000p-5f},{0x1.feca9c0000000p-1f,-0x1.1943e40000000p-4f},
{0x1.fe6bf20000000p-1f,-0x1.415e540000000p-4f},{0x1.fe00b20000000p-1f,-0x1.6970d40000000p-4f},{0x1.fd88da0000000p-1f,-0x1.917a6c0000000p-4f},{0x1.fd04700000000p-1f,-0x1.b97a1a0000000p-4f},
{0x1.fc73780000000p-1f,-0x1.e16ee40000000p-4f},{0x1.fbd5f40000000p-1f,-0x1.04abe80000000p-3f},{0x1.fb2be80000000p-1f,-0x1.1899ee0000000p-3f},{0x1.fa75580000000p-1f,-0x1.2c81060000000p-3f},
{0x1.f9b24a0000000p-1f,-0x1.4060b60000000p-3f},{0x1.f8e2c00000000p-1f,-0x1.54387e0000000p-3f},{0x1.f806c20000000p-1f,-0x1.6807e20000000p-3f},{0x1.f71e540000000p-1f,-0x1.7bce620000000p-3f},
{0x1.f6297c0000000p-1f,-0x1.8f8b840000000p-3f},{0x1.f528420000000p-1f,-0x1.a33eca0000000p-3f},{0x1.f41aa80000000p-1f,-0x1.b6e7b80000000p-3f},{0x1.f300b80000000p-1f,-0x1.ca85d20000000p-3f},
{0x1.f1da780000000p-1f,-0x1.de189a0000000p-3f},{0x1.f0a7f00000000p-1f,-0x1.f19f980000000p-3f},{0x1.ef69260000000p-1f,-0x1.028d260000000p-2f},{0x1.ee1e220000000p-1f,-0x1.0c44200000000p-2f},
{0x1.ecc6ee0000000p-1f,-0x1.15f47c0000000p-2f},{0x1.eb63920000000p-1f,-0x1.1f9dfc0000000p-2f},{0x1.e9f4160000000p-1f,-0x1.2940620000000p-2f},{0x1.e878820000000p-1f,-0x1.32db740000000p-2f},
{0x1.e6f0e20000000p-1f,-0x1.3c6ef40000000p-2f},{0x1.e55d3c0000000p-1f,-0x1.45faa40000000p-2f},{0x1.e3bd9e0000000p-1f,-0x1.4f7e4a0000000p-2f},{0x1.e212100000000p-1f,-0x1.58f9a80000000p-2f},
{0x1.e05a9e0000000p-1f,-0x1.626c820000000p-2f},{0x1.de97500000000p-1f,-0x1.6bd69e0000000p-2f},{0x1.dcc8340000000p-1f,-0x1.7537c20000000p-2f},{0x1.daed540000000p-1f,-0x1.7e8fae0000000p-2f},
{0x1.d906bc0000000p-1f,-0x1.87de2a0000000p-2f},{0x1.d7147a0000000p-1f,-0x1.9122fc0000000p-2f},{0x1.d516960000000p-1f,-0x1.9a5de60000000p-2f},{0x1.d30d200000000p-1f,-0x1.a38eb20000000p-2f},
{0x1.d0f8240000000p-1f,-0x1.acb5240000000p-2f},{0x1.ced7b00000000p-1f,-0x1.b5d1000000000p-2f},{0x1.ccabce0000000p-1f,-0x1.bee2100000000p-2f},{0x1.ca74900000000p-1f,-0x1.c7e81a0000000p-2f},
{0x1.c832020000000p-1f,-0x1.d0e2e20000000p-2f},{0x1.c5e4320000000p-1f,-0x1.d9d2340000000p-2f},{0x1.c38b300000000p-1f,-0x1.e2b5d40000000p-2f},{0x1.c127080000000p-1f,-0x1.eb8d8a0000000p-2f},
{0x1.beb7cc0000000p-1f,-0x1.f459200000000p-2f},{0x1.bc3d8a0000000p-1f,-0x1.fd185e0000000p-2f},{0x1.b9b8520000000p-1f,-0x1.02e5860000000p-1f},{0x1.b728340000000p-1f,-0x1.07387a0000000p-1f},
{0x1.b48d400000000p-1f,-0x1.0b84ee0000000p-1f},{0x1.b1e7880000000p-1f,-0x1.0fcaca0000000p-1f},{0x1.af37180000000p-1f,-0x1.1409f00000000p-1f},{0x1.ac7c060000000p-1f,-0x1.1842480000000p-1f},
{0x1.a9b6620000000p-1f,-0x1.1c73b40000000p-1f},{0x1.a6e63e0000000p-1f,-0x1.209e1c0000000p-1f},{0x1.a40ba80000000p-1f,-0x1.24c1640000000p-1f},{0x1.a126b60000000p-1f,-0x1.28dd740000000p-1f},
{0x1.9e377a0000000p-1f,-0x1.2cf2300000000p-1f},{0x1.9b3e040000000p-1f,-0x1.30ff800000000p-1f},{0x1.983a6a0000000p-1f,-0x1.3505480000000p-1f},{0x1.952cbc0000000p-1f,-0x1.3903720000000p-1f},
{0x1.9215100000000p-1f,-0x1.3cf9e20000000p-1f},{0x1.8ef3780000000p-1f,-0x1.40e8800000000p-1f},{0x1.8bc8060000000p-1f,-0x1.44cf320000000p-1f},{0x1.8892d20000000p-1f,-0x1.48ade20000000p-1f},
{0x1.8553ee0000000p-1f,-0x1.4c84740000000p-1f},{0x1.820b700000000p-1f,-0x1.5052d40000000p-1f},{0x1.7eb96a0000000p-1f,-0x1.5418e60000000p-1f},{0x1.7b5df20000000p-1f,-0x1.57d6940000000p-1f},
{0x1.77f91e0000000p-1f,-0x1.5b8bc60000000p-1f},{0x1.748b040000000p-1f,-0x1.5f38640000000p-1f},{0x1.7113b80000000p-1f,-0x1.62dc580000000p-1f},{0x1.6d93520000000p-1f,-0x1.66778c0000000p-1f},
{0x1.6a09e60000000p-1f,-0x1.6a09e60000000p-1f},{0x1.66778c0000000p-1f,-0x1.6d93520000000p-1f},{0x1.62dc580000000p-1f,-0x1.7113b80000000p-1f},{0x1.5f38640000000p-1f,-0x1.748b040000000p-1f},
{0x1.5b8bc60000000p-1f,-0x1.77f91e0000000p-1f},{0x1.57d6940000000p-1f,-0x1.7b5df20000000p-1f},{0x1.5418e60000000p-1f,-0x1.7eb96a0000000p-1f},{0x1.5052d40000000p-1f,-0x1.820b700000000p-1f},
{0x1.4c84740000000p-1f,-0x1.8553ee0000000p-1f},{0x1.48ade20000000p-1f,-0x1.8892d20000000p-1f},{0x1.44cf320000000p-1f,-0x1.8bc8060000000p-1f},{0x1.40e8800000000p-1f,-0x1.8ef3780000000p-1f},
{0x1.3cf9e20000000p-1f,-0x1.9215100000000p-1f},{0x1.3903720000000p-1f,-0x1.952cbc0000000p-1f},{0x1.3505480000000p-1f,-0x1.983a6a0000000p-1f},{0x1.30ff800000000p-1f,-0x1.9b3e040000000p-1f},
{0x1.2cf2300000000p-1f,-0x1.9e377a0000000p-1f},{0x1.28dd740000000p-1f,-0x1.a126b60000000p-1f},{0x1.24c1640000000p-1f,-0x1.a40ba80000000p-1f},{0x1.209e1c0000000p-1f,-0x1.a6e63e0000000p-1f},
{0x1.1c73b40000000p-1f,-0x1.a9b6620000000p-1f},{0x1.1842480000000p-1f,-0x1.ac7c060000000p-1f},{0x1.1409f00000000p-1f,-0x1.af37180000000p-1f},{0x1.0fcaca0000000p-1f,-0x1.b1e7880000000p-1f},
{0x1.0b84ee0000000p-1f,-0x1.b48d400000000p-1f},{0x1.07387a0000000p-1f,-0x1.b728340000000p-1f},{0x1.02e5860000000p-1f,-0x1.b9b8520000000p-1f},{0x1.fd185e0000000p-2f,-0x1.bc3d8a0000000p-1f},
{0x1.f459200000000p-2f,-0x1.beb7cc0000000p-1f},{0x1.eb8d8a0000000p-2f,-0x1.c127080000000p-1f},{0x1.e2b5d40000000p-2f,-0x1.c38b300000000p-1f},{0x1.d9d2340000000p-2f,-0x1.c5e4320000000p-1f},
{0x1.d0e2e20000000p-2f,-0x1.c832020000000p-1f},{0x1.c7e81a0000000p-2f,-0x1.ca74900000000p-1f},{0x1.bee2100000000p-2f,-0x1.ccabce0000000p-1f},{0x1.b5d1000000000p-2f,-0x1.ced7b00000000p-1f},
{0x1.acb5240000000p-2f,-0x1.d0f8240000000p-1f},{0x1.a38eb20000000p-2f,-0x1.d30d200000000p-1f},{0x1.9a5de60000000p-2f,-0x1.d516960000000p-1f},{0x1.9122fc0000000p-2f,-0x1.d7147a0000000p-1f},
{0x1.87de2a0000000p-2f,-0x1.d906bc0000000p-1f},{0x1.7e8fae0000000p-2f,-0x1.daed540000000p-1f},{0x1.7537c20000000p-2f,-0x1.dcc8340000000p-1f},{0x1.6bd69e0000000p-2f,-0x1.de97500000000p-1f},
{0x1.626c820000000p-2f,-0x1.e05a9e0000000p-1f},{0x1.58f9a80000000p-2f,-0x1.e212100000000p-1f},{0x1.4f7e4a0000000p-2f,-0x1.e3bd9e0000000p-1f},{0x1.45faa40000000p-2f,-0x1.e55d3c0000000p-1f},
{0x1.3c6ef40000000p-2f,-0x1.e6f0e20000000p-1f},{0x1.32db740000000p-2f,-0x1.e878820000000p-1f},{0x1.2940620000000p-2f,-0x1.e9f4160000000p-1f},{0x1.1f9dfc0000000p-2f,-0x1.eb63920000000p-1f},
{0x1.15f47c0000000p-2f,-0x1.ecc6ee0000000p-1f},{0x1.0c44200000000p-2f,-0x1.ee1e220000000p-1f},{0x1.028d260000000p-2f,-0x1.ef69260000000p-1f},{0x1.f19f980000000p-3f,-0x1.f0a7f00000000p-1f},
{0x1.de189a0000000p-3f,-0x1.f1da780000000p-1f},{0x1.ca85d20000000p-3f,-0x1.f300b80000000p-1f},{0x1.b6e7b80000000p-3f,-0x1.f41aa80000000p-1f},{0x1.a33eca0000000p-3f,-0x1.f528420000000p-1f},
{0x1.8f8b840000000p-3f,-0x1.f6297c0000000p-1f},{0x1.7bce620000000p-3f,-0x1.f71e540000000p-1f},{0x1.6807e20000000p-3f,-0x1.f806c20000000p-1f},{0x1.54387e0000000p-3f,-0x1.f8e2c00000000p-1f},
{0x1.4060b60000000p-3f,-0x1.f9b24a0000000p-1f},{0x1.2c81060000000p-3f,-0x1.fa75580000000p-1f},{0x1.1899ee0000000p-3f,-0x1.fb2be80000000p-1f},{0x1.04abe80000000p-3f,-0x1.fbd5f40000000p-1f},
{0x1.e16ee40000000p-4f,-0x1.fc73780000000p-1f},{0x1.b97a1a0000000p-4f,-0x1.fd04700000000p-1f},{0x1.917a6c0000000p-4f,-0x1.fd88da0000000p-1f},{0x1.6970d40000000p-4f,-0x1.fe00b20000000p-1f},
{0x1.415e540000000p-4f,-0x1.fe6bf20000000p-1f},{0x1.1943e40000000p-4f,-0x1.feca9c0000000p-1f},{0x1.e245060000000p-5f,-0x1.ff1cac0000000p-1f},{0x1.91f6600000000p-5f,-0x1.ff621e0000000p-1f},
{0x1.419dce0000000p-5f,-0x1.ff9af20000000p-1f},{0x1.e27a980000000p-6f,-0x1.ffc7280000000p-1f},{0x1.41adac0000000p-6f,-0x1.ffe6bc0000000p-1f},{0x1.41b1a40000000p-7f,-0x1.fff9ae0000000p-1f},
{0x0.0p+0f,-0x1.0000000000000p+0f},{-0x1.41b1a40000000p-7f,-0x1.fff9ae0000000p-1f},{-0x1.41adac0000000p-6f,-0x1.ffe6bc0000000p-1f},{-0x1.e27a980000000p-6f,-0x1.ffc7280000000p-1f},
{-0x1.419dce0000000p-5f,-0x1.ff9af20000000p-1f},{-0x1.91f6600000000p-5f,-0x1.ff621e0000000p-1f},{-0x1.e245060000000p-5f,-0x1.ff1cac0000000p-1f},{-0x1.1943e40000000p-4f,-0x1.feca9c0000000p-1f},
{-0x1.415e540000000p-4f,-0x1.fe6bf20000000p-1f},{-0x1.6970d40000000p-4f,-0x1.fe00b20000000p-1f},{-0x1.917a6c0000000p-4f,-0x1.fd88da0000000p-1f},{-0x1.b97a1a0000000p-4f,-0x1.fd04700000000p-1f},
{-0x1.e16ee40000000p-4f,-0x1.fc73780000000p-1f},{-0x1.04abe80000000p-3f,-0x1.fbd5f40000000p-1f},{-0x1.1899ee0000000p-3f,-0x1.fb2be80000000p-1f},{-0x1.2c81060000000p-3f,-0x1.fa75580000000p-1f},
{-0x1.4060b60000000p-3f,-0x1.f9b24a0000000p-1f},{-0x1.54387e0000000p-3f,-0x1.f8e2c00000000p-1f},{-0x1.6807e20000000p-3f,-0x1.f806c20000000p-1f},{-0x1.7bce620000000p-3f,-0x1.f71e540000000p-1f},
{-0x1.8f8b840000000p-3f,-0x1.f6297c0000000p-1f},{-0x1.a33eca0000000p-3f,-0x1.f528420000000p-1f},{-0x1.b6e7b80000000p-3f,-0x1.f41aa80000000p-1f},{-0x1.ca85d20000000p-3f,-0x1.f300b80000000p-1f},
{-0x1.de189a0000000p-3f,-0x1.f1da780000000p-1f},{-0x1.f19f980000000p-3f,-0x1.f0a7f00000000p-1f},{-0x1.028d260000000p-2f,-0x1.ef69260000000p-1f},{-0x1.0c44200000000p-2f,-0x1.ee1e220000000p-1f},
{-0x1.15f47c0000000p-2f,-0x1.ecc6ee0000000p-1f},{-0x1.1f9dfc0000000p-2f,-0x1.eb63920000000p-1f},{-0x1.2940620000000p-2f,-0x1.e9f4160000000p-1f},{-0x1.32db740000000p-2f,-0x1.e878820000000p-1f},
{-0x1.3c6ef40000000p-2f,-0x1.e6f0e20000000p-1f},{-0x1.45faa40000000p-2f,-0x1.e55d3c0000000p-1f},{-0x1.4f7e4a0000000p-2f,-0x1.e3bd9e0000000p-1f},{-0x1.58f9a80000000p-2f,-0x1.e212100000000p-1f},
{-0x1.626c820000000p-2f,-0x1.e05a9e0000000p-1f},{-0x1.6bd69e0000000p-2f,-0x1.de97500000000p-1f},{-0x1.7537c20000000p-2f,-0x1.dcc8340000000p-1f},{-0x1.7e8fae0000000p-2f,-0x1.daed540000000p-1f},
{-0x1.87de2a0000000p-2f,-0x1.d906bc0000000p-1f},{-0x1.9122fc0000000p-2f,-0x1.d7147a0000000p-1f},{-0x1.9a5de60000000p-2f,-0x1.d516960000000p-1f},{-0x1.a38eb20000000p-2f,-0x1.d30d200000000p-1f},
{-0x1.acb5240000000p-2f,-0x1.d0f8240000000p-1f},{-0x1.b5d1000000000p-2f,-0x1.ced7b00000000p-1f},{-0x1.bee2100000000p-2f,-0x1.ccabce0000000p-1f},{-0x1.c7e81a0000000p-2f,-0x1.ca74900000000p-1f},
{-0x1.d0e2e20000000p-2f,-0x1.c832020000000p-1f},{-0x1.d9d2340000000p-2f,-0x1.c5e4320000000p-1f},{-0x1.e2b5d40000000p-2f,-0x1.c38b300000000p-1f},{-0x1.eb8d8a0000000p-2f,-0x1.c127080000000p-1f},
{-0x1.f459200000000p-2f,-0x1.beb7cc0000000p-1f},{-0x1.fd185e0000000p-2f,-0x1.bc3d8a0000000p-1f},{-0x1.02e5860000000p-1f,-0x1.b9b8520000000p-1f},{-0x1.07387a0000000p-1f,-0x1.b728340000000p-1f},
{-0x1.0b84ee0000000p-1f,-0x1.b48d400000000p-1f},{-0x1.0fcaca0000000p-1f,-0x1.b1e7880000000p-1f},{-0x1.1409f00000000p-1f,-0x1.af37180000000p-1f},{-0x1.1842480000000p-1f,-0x1.ac7c060000000p-1f},
{-0x1.1c73b40000000p-1f,-0x1.a9b6620000000p-1f},{-0x1.209e1c0000000p-1f,-0x1.a6e63e0000000p-1f},{-0x1.24c1640000000p-1f,-0x1.a40ba80000000p-1f},{-0x1.28dd740000000p-1f,-0x1.a126b60000000p-1f},
{-0x1.2cf2300000000p-1f,-0x1.9e377a0000000p-1f},{-0x1.30ff800000000p-1f,-0x1.9b3e040000000p-1f},{-0x1.3505480000000p-1f,-0x1.983a6a0000000p-1f},{-0x1.3903720000000p-1f,-0x1.952cbc0000000p-1f},
{-0x1.3cf9e20000000p-1f,-0x1.9215100000000p-1f},{-0x1.40e8800000000p-1f,-0x1.8ef3780000000p-1f},{-0x1.44cf320000000p-1f,-0x1.8bc8060000000p-1f},{-0x1.48ade20000000p-1f,-0x1.8892d20000000p-1f},
{-0x1.4c84740000000p-1f,-0x1.8553ee0000000p-1f},{-0x1.5052d40000000p-1f,-0x1.820b700000000p-1f},{-0x1.5418e60000000p-1f,-0x1.7eb96a0000000p-1f},{-0x1.57d6940000000p-1f,-0x1.7b5df20000000p-1f},
{-0x1.5b8bc60000000p-1f,-0x1.77f91e0000000p-1f},{-0x1.5f38640000000p-1f,-0x1.748b040000000p-1f},{-0x1.62dc580000000p-1f,-0x1.7113b80000000p-1f},{-0x1.66778c0000000p-1f,-0x1.6d93520000000p-1f},
{-0x1.6a09e60000000p-1f,-0x1.6a09e60000000p-1f},{-0x1.6d93520000000p-1f,-0x1.66778c0000000p-1f},{-0x1.7113b80000000p-1f,-0x1.62dc580000000p-1f},{-0x1.748b040000000p-1f,-0x1.5f38640000000p-1f},
{-0x1.77f91e0000000p-1f,-0x1.5b8bc60000000p-1f},{-0x1.7b5df20000000p-1f,-0x1.57d6940000000p-1f},{-0x1.7eb96a0000000p-1f,-0x1.5418e60000000p-1f},{-0x1.820b700000000p-1f,-0x1.5052d40000000p-1f},
{-0x1.8553ee0000000p-1f,-0x1.4c84740000000p-1f},{-0x1.8892d20000000p-1f,-0x1.48ade20000000p-1f},{-0x1.8bc8060000000p-1f,-0x1.44cf320000000p-1f},{-0x1.8ef3780000000p-1f,-0x1.40e8800000000p-1f},
{-0x1.9215100000000p-1f,-0x1.3cf9e20000000p-1f},{-0x1.952cbc0000000p-1f,-0x1.3903720000000p-1f},{-0x1.983a6a0000000p-1f,-0x1.3505480000000p-1f},{-0x1.9b3e040000000p-1f,-0x1.30ff800000000p-1f},
{-0x1.9e377a0000000p-1f,-0x1.2cf2300000000p-1f},{-0x1.a126b60000000p-1f,-0x1.28dd740000000p-1f},{-0x1.a40ba80000000p-1f,-0x1.24c1640000000p-1f},{-0x1.a6e63e0000000p-1f,-0x1.209e1c0000000p-1f},
{-0x1.a9b6620000000p-1f,-0x1.1c73b40000000p-1f},{-0x1.ac7c060000000p-1f,-0x1.1842480000000p-1f},{-0x1.af37180000000p-1f,-0x1.1409f00000000p-1f},{-0x1.b1e7880000000p-1f,-0x1.0fcaca0000000p-1f},
{-0x1.b48d400000000p-1f,-0x1.0b84ee0000000p-1f},{-0x1.b728340000000p-1f,-0x1.07387a0000000p-1f},{-0x1.b9b8520000000p-1f,-0x1.02e5860000000p-1f},{-0x1.bc3d8a0000000p-1f,-0x1.fd185e0000000p-2f},
{-0x1.beb7cc0000000p-1f,-0x1.f459200000000p-2f},{-0x1.c127080000000p-1f,-0x1.eb8d8a0000000p-2f},{-0x1.c38b300000000p-1f,-0x1.e2b5d40000000p-2f},{-0x1.c5e4320000000p-1f,-0x1.d9d2340000000p-2f},
{-0x1.c832020000000p-1f,-0x1.d0e2e20000000p-2f},{-0x1.ca74900000000p-1f,-0x1.c7e81a0000000p-2f},{-0x1.ccabce0000000p-1f,-0x1.bee2100000000p-2f},{-0x1.ced7b00000000p-1f,-0x1.b5d1000000000p-2f},
{-0x1.d0f8240000000p-1f,-0x1.acb5240000000p-2f},{-0x1.d30d200000000p-1f,-0x1.a38eb20000000p-2f},{-0x1.d516960000000p-1f,-0x1.9a5de60000000p-2f},{-0x1.d7147a0000000p-1f,-0x1.9122fc0000000p-2f},
{-0x1.d906bc0000000p-1f,-0x1.87de2a0000000p-2f},{-0x1.daed540000000p-1f,-0x1.7e8fae0000000p-2f},{-0x1.dcc8340000000p-1f,-0x1.7537c20000000p-2f},{-0x1.de97500000000p-1f,-0x1.6bd69e0000000p-2f},
{-0x1.e05a9e0000000p-1f,-0x1.626c820000000p-2f},{-0x1.e212100000000p-1f,-0x1.58f9a80000000p-2f},{-0x1.e3bd9e0000000p-1f,-0x1.4f7e4a0000000p-2f},{-0x1.e55d3c0000000p-1f,-0x1.45faa40000000p-2f},
{-0x1.e6f0e20000000p-1f,-0x1.3c6ef40000000p-2f},{-0x1.e878820000000p-1f,-0x1.32db740000000p-2f},{-0x1.e9f4160000000p-1f,-0x1.2940620000000p-2f},{-0x1.eb63920000000p-1f,-0x1.1f9dfc0000000p-2f},
{-0x1.ecc6ee0000000p-1f,-0x1.15f47c0000000p-2f},{-0x1.ee1e220000000p-1f,-0x1.0c44200000000p-2f},{-0x1.ef69260000000p-1f,-0x1.028d260000000p-2f},{-0x1.f0a7f00000000p-1f,-0x1.f19f980000000p-3f},
{-0x1.f1da780000000p-1f,-0x1.de189a0000000p-3f},{-0x1.f300b80000000p-1f,-0x1.ca85d20000000p-3f},{-0x1.f41aa80000000p-1f,-0x1.b6e7b80000000p-3f},{-0x1.f528420000000p-1f,-0x1.a33eca0000000p-3f},
{-0x1.f6297c0000000p-1f,-0x1.8f8b840000000p-3f},{-0x1.f71e540000000p-1f,-0x1.7bce620000000p-3f},{-0x1.f806c20000000p-1f,-0x1.6807e20000000p-3f},{-0x1.f8e2c00000000p-1f,-0x1.54387e0000000p-3f},
{-0x1.f9b24a0000000p-1f,-0x1.4060b60000000p-3f},{-0x1.fa75580000000p-1f,-0x1.2c81060000000p-3f},{-0x1.fb2be80000000p-1f,-0x1.1899ee0000000p-3f},{-0x1.fbd5f40000000p-1f,-0x1.04abe80000000p-3f},
{-0x1.fc73780000000p-1f,-0x1.e16ee40000000p-4f},{-0x1.fd04700000000p-1f,-0x1.b97a1a0000000p-4f},{-0x1.fd88da0000000p-1f,-0x1.917a6c0000000p-4f},{-0x1.fe00b20000000p-1f,-0x1.6970d40000000p-4f},
{-0x1.fe6bf20000000p-1f,-0x1.415e540000000p-4f},{-0x1.feca9c0000000p-1f,-0x1.1943e40000000p-4f},{-0x1.ff1cac0000000p-1f,-0x1.e245060000000p-5f},{-0x1.ff621e0000000p-1f,-0x1.91f6600000000p-5f},
{-0x1.ff9af20000000p-1f,-0x1.419dce0000000p-5f},{-0x1.ffc7280000000p-1f,-0x1.e27a980000000p-6f},{-0x1.ffe6bc0000000p-1f,-0x1.41adac0000000p-6f},{-0x1.fff9ae0000000p-1f,-0x1.41b1a40000000p-7f},
{-0x1.0000000000000p+0f,0x0.0p+0f},{-0x1.fff9ae0000000p-1f,0x1.41b1a40000000p-7f},{-0x1.ffe6bc0000000p-1f,0x1.41adac0000000p-6f},{-0x1.ffc7280000000p-1f,0x1.e27a980000000p-6f},
{-0x1.ff9af20000000p-1f,0x1.419dce0000000p-5f},{-0x1.ff621e0000000p-1f,0x1.91f6600000000p-5f},{-0x1.ff1cac0000000p-1f,0x1.e245060000000p-5f},{-0x1.feca9c0000000p-1f,0x1.1943e40000000p-4f},
{-0x1.fe6bf20000000p-1f,0x1.415e540000000p-4f},{-0x1.fe00b20000000p-1f,0x1.6970d40000000p-4f},{-0x1.fd88da0000000p-1f,0x1.917a6c0000000p-4f},{-0x1.fd04700000000p-1f,0x1.b97a1a0000000p-4f},
{-0x1.fc73780000000p-1f,0x1.e16ee40000000p-4f},{-0x1.fbd5f40000000p-1f,0x1.04abe80000000p-3f},{-0x1.fb2be80000000p-1f,0x1.1899ee0000000p-3f},{-0x1.fa75580000000p-1f,0x1.2c81060000000p-3f},
{-0x1.f9b24a0000000p-1f,0x1.4060b60000000p-3f},{-0x1.f8e2c00000000p-1f,0x1.54387e0000000p-3f},{-0x1.f806c20000000p-1f,0x1.6807e20000000p-3f},{-0x1.f71e540000000p-1f,0x1.7bce620000000p-3f},
{-0x1.f6297c0000000p-1f,0x1.8f8b840000000p-3f},{-0x1.f528420000000p-1f,0x1.a33eca0000000p-3f},{-0x1.f41aa80000000p-1f,0x1.b6e7b80000000p-3f},{-0x1.f300b80000000p-1f,0x1.ca85d20000000p-3f},
{-0x1.f1da780000000p-1f,0x1.de189a0000000p-3f},{-0x1.f0a7f00000000p-1f,0x1.f19f980000000p-3f},{-0x1.ef69260000000p-1f,0x1.028d260000000p-2f},{-0x1.ee1e220000000p-1f,0x1.0c44200000000p-2f},
{-0x1.ecc6ee0000000p-1f,0x1.15f47c0000000p-2f},{-0x1.eb63920000000p-1f,0x1.1f9dfc0000000p-2f},{-0x1.e9f4160000000p-1f,0x1.2940620000000p-2f},{-0x1.e878820000000p-1f,0x1.32db740000000p-2f},
{-0x1.e6f0e20000000p-1f,0x1.3c6ef40000000p-2f},{-0x1.e55d3c0000000p-1f,0x1.45faa40000000p-2f},{-0x1.e3bd9e0000000p-1f,0x1.4f7e4a0000000p-2f},{-0x1.e212100000000p-1f,0x1.58f9a80000000p-2f},
{-0x1.e05a9e0000000p-1f,0x1.626c820000000p-2f},{-0x1.de97500000000p-1f,0x1.6bd69e0000000p-2f},{-0x1.dcc8340000000p-1f,0x1.7537c20000000p-2f},{-0x1.daed540000000p-1f,0x1.7e8fae0000000p-2f},
{-0x1.d906bc0000000p-1f,0x1.87de2a0000000p-2f},{-0x1.d7147a0000000p-1f,0x1.9122fc0000000p-2f},{-0x1.d516960000000p-1f,0x1.9a5de60000000p-2f},{-0x1.d30d200000000p-1f,0x1.a38eb20000000p-2f},
{-0x1.d0f8240000000p-1f,0x1.acb5240000000p-2f},{-0x1.ced7b00000000p-1f,0x1.b5d1000000000p-2f},{-0x1.ccabce0000000p-1f,0x1.bee2100000000p-2f},{-0x1.ca74900000000p-1f,0x1.c7e81a0000000p-2f},
{-0x1.c832020000000p-1f,0x1.d0e2e20000000p-2f},{-0x1.c5e4320000000p-1f,0x1.d9d2340000000p-2f},{-0x1.c38b300000000p-1f,0x1.e2b5d40000000p-2f},{-0x1.c127080000000p-1f,0x1.eb8d8a0000000p-2f},
{-0x1.beb7cc0000000p-1f,0x1.f459200000000p-2f},{-0x1.bc3d8a0000000p-1f,0x1.fd185e0000000p-2f},{-0x1.b9b8520000000p-1f,0x1.02e5860000000p-1f},{-0x1.b728340000000p-1f,0x1.07387a0000000p-1f},
{-0x1.b48d400000000p-1f,0x1.0b84ee0000000p-1f},{-0x1.b1e7880000000p-1f,0x1.0fcaca0000000p-1f},{-0x1.af37180000000p-1f,0x1.1409f00000000p-1f},{-0x1.ac7c060000000p-1f,0x1.1842480000000p-1f},
{-0x1.a9b6620000000p-1f,0x1.1c73b40000000p-1f},{-0x1.a6e63e0000000p-1f,0x1.209e1c0000000p-1f},{-0x1.a40ba80000000p-1f,0x1.24c1640000000p-1f},{-0x1.a126b60000000p-1f,0x1.28dd740000000p-1f},
{-0x1.9e377a0000000p-1f,0x1.2cf2300000000p-1f},{-0x1.9b3e040000000p-1f,0x1.30ff800000000p-1f},{-0x1.983a6a0000000p-1f,0x1.3505480000000p-1f},{-0x1.952cbc0000000p-1f,0x1.3903720000000p-1f},
{-0x1.9215100000000p-1f,0x1.3cf9e20000000p-1f},{-0x1.8ef3780000000p-1f,0x1.40e8800000000p-1f},{-0x1.8bc8060000000p-1f,0x1.44cf320000000p-1f},{-0x1.8892d20000000p-1f,0x1.48ade20000000p-1f},
{-0x1.8553ee0000000p-1f,0x1.4c84740000000p-1f},{-0x1.820b700000000p-1f,0x1.5052d40000000p-1f},{-0x1.7eb96a0000000p-1f,0x1.5418e60000000p-1f},{-0x1.7b5df20000000p-1f,0x1.57d6940000000p-1f},
{-0x1.77f91e0000000p-1f,0x1.5b8bc60000000p-1f},{-0x1.748b040000000p-1f,0x1.5f38640000000p-1f},{-0x1.7113b80000000p-1f,0x1.62dc580000000p-1f},{-0x1.6d93520000000p-1f,0x1.66778c0000000p-1f},
{-0x1.6a09e60000000p-1f,0x1.6a09e60000000p-1f},{-0x1.66778c0000000p-1f,0x1.6d93520000000p-1f},{-0x1.62dc580000000p-1f,0x1.7113b80000000p-1f},{-0x1.5f38640000000p-1f,0x1.748b040000000p-1f},
{-0x1.5b8bc60000000p-1f,0x1.77f91e0000000p-1f},{-0x1.57d6940000000p-1f,0x1.7b5df20000000p-1f},{-0x1.5418e60000000p-1f,0x1.7eb96a0000000p-1f},{-0x1.5052d40000000p-1f,0x1.820b700000000p-1f},
{-0x1.4c84740000000p-1f,0x1.8553ee0000000p-1f},{-0x1.48ade20000000p-1f,0x1.8892d20000000p-1f},{-0x1.44cf320000000p-1f,0x1.8bc8060000000p-1f},{-0x1.40e8800000000p-1f,0x1.8ef3780000000p-1f},
{-0x1.3cf9e20000000p-1f,0x1.9215100000000p-1f},{-0x1.3903720000000p-1f,0x1.952cbc0000000p-1f},{-0x1.3505480000000p-1f,0x1.983a6a0000000p-1f},{-0x1.30ff800000000p-1f,0x1.9b3e040000000p-1f},
{-0x1.2cf2300000000p-1f,0x1.9e377a0000000p-1f},{-0x1.28dd740000000p-1f,0x1.a126b60000000p-1f},{-0x1.24c1640000000p-1f,0x1.a40ba80000000p-1f},{-0x1.209e1c0000000p-1f,0x1.a6e63e0000000p-1f},
{-0x1.1c73b40000000p-1f,0x1.a9b6620000000p-1f},{-0x1.1842480000000p-1f,0x1.ac7c060000000p-1f},{-0x1.1409f00000000p-1f,0x1.af37180000000p-1f},{-0x1.0fcaca0000000p-1f,0x1.b1e7880000000p-1f},
{-0x1.0b84ee0000000p-1f,0x1.b48d400000000p-1f},{-0x1.07387a0000000p-1f,0x1.b728340000000p-1f},{-0x1.02e5860000000p-1f,0x1.b9b8520000000p-1f},{-0x1.fd185e0000000p-2f,0x1.bc3d8a0000000p-1f},
{-0x1.f459200000000p-2f,0x1.beb7cc0000000p-1f},{-0x1.eb8d8a0000000p-2f,0x1.c127080000000p-1f},{-0x1.e2b5d40000000p-2f,0x1.c38b300000000p-1f},{-0x1.d9d2340000000p-2f,0x1.c5e4320000000p-1f},
{-0x1.d0e2e20000000p-2f,0x1.c832020000000p-1f},{-0x1.c7e81a0000000p-2f,0x1.ca74900000000p-1f},{-0x1.bee2100000000p-2f,0x1.ccabce0000000p-1f},{-0x1.b5d1000000000p-2f,0x1.ced7b00000000p-1f},
{-0x1.acb5240000000p-2f,0x1.d0f8240000000p-1f},{-0x1.a38eb20000000p-2f,0x1.d30d200000000p-1f},{-0x1.9a5de60000000p-2f,0x1.d516960000000p-1f},{-0x1.9122fc0000000p-2f,0x1.d7147a0000000p-1f},
{-0x1.87de2a0000000p-2f,0x1.d906bc0000000p-1f},{-0x1.7e8fae0000000p-2f,0x1.daed540000000p-1f},{-0x1.7537c20000000p-2f,0x1.dcc8340000000p-1f},{-0x1.6bd69e0000000p-2f,0x1.de97500000000p-1f},
{-0x1.626c820000000p-2f,0x1.e05a9e0000000p-1f},{-0x1.58f9a80000000p-2f,0x1.e212100000000p-1f},{-0x1.4f7e4a0000000p-2f,0x1.e3bd9e0000000p-1f},{-0x1.45faa40000000p-2f,0x1.e55d3c0000000p-1f},
{-0x1.3c6ef40000000p-2f,0x1.e6f0e20000000p-1f},{-0x1.32db740000000p-2f,0x1.e878820000000p-1f},{-0x1.2940620000000p-2f,0x1.e9f4160000000p-1f},{-0x1.1f9dfc0000000p-2f,0x1.eb63920000000p-1f},
{-0x1.15f47c0000000p-2f,0x1.ecc6ee0000000p-1f},{-0x1.0c44200000000p-2f,0x1.ee1e220000000p-1f},{-0x1.028d260000000p-2f,0x1.ef69260000000p-1f},{-0x1.f19f980000000p-3f,0x1.f0a7f00000000p-1f},
{-0x1.de189a0000000p-3f,0x1.f1da780000000p-1f},{-0x1.ca85d20000000p-3f,0x1.f300b80000000p-1f},{-0x1.b6e7b80000000p-3f,0x1.f41aa80000000p-1f},{-0x1.a33eca0000000p-3f,0x1.f528420000000p-1f},
{-0x1.8f8b840000000p-3f,0x1.f6297c0000000p-1f},{-0x1.7bce620000000p-3f,0x1.f71e540000000p-1f},{-0x1.6807e20000000p-3f,0x1.f806c20000000p-1f},{-0x1.54387e0000000p-3f,0x1.f8e2c00000000p-1f},
{-0x1.4060b60000000p-3f,0x1.f9b24a0000000p-1f},{-0x1.2c81060000000p-3f,0x1.fa75580000000p-1f},{-0x1.1899ee0000000p-3f,0x1.fb2be80000000p-1f},{-0x1.04abe80000000p-3f,0x1.fbd5f40000000p-1f},
{-0x1.e16ee40000000p-4f,0x1.fc73780000000p-1f},{-0x1.b97a1a0000000p-4f,0x1.fd04700000000p-1f},{-0x1.917a6c0000000p-4f,0x1.fd88da0000000p-1f},{-0x1.6970d40000000p-4f,0x1.fe00b20000000p-1f},
{-0x1.415e540000000p-4f,0x1.fe6bf20000000p-1f},{-0x1.1943e40000000p-4f,0x1.feca9c0000000p-1f},{-0x1.e245060000000p-5f,0x1.ff1cac0000000p-1f},{-0x1.91f6600000000p-5f,0x1.ff621e0000000p-1f},
{-0x1.419dce0000000p-5f,0x1.ff9af20000000p-1f},{-0x1.e27a980000000p-6f,0x1.ffc7280000000p-1f},{-0x1.41adac0000000p-6f,0x1.ffe6bc0000000p-1f},{-0x1.41b1a40000000p-7f,0x1.fff9ae0000000p-1f},
{0x0.0p+0f,0x1.0000000000000p+0f},{0x1.41b1a40000000p-7f,0x1.fff9ae0000000p-1f},{0x1.41adac0000000p-6f,0x1.ffe6bc0000000p-1f},{0x1.e27a980000000p-6f,0x1.ffc7280000000p-1f},
{0x1.419dce0000000p-5f,0x1.ff9af20000000p-1f},{0x1.91f6600000000p-5f,0x1.ff621e0000000p-1f},{0x1.e245060000000p-5f,0x1.ff1cac0000000p-1f},{0x1.1943e40000000p-4f,0x1.feca9c0000000p-1f},
{0x1.415e540000000p-4f,0x1.fe6bf20000000p-1f},{0x1.6970d40000000p-4f,0x1.fe00b20000000p-1f},{0x1.917a6c0000000p-4f,0x1.fd88da0000000p-1f},{0x1.b97a1a0000000p-4f,0x1.fd04700000000p-1f},
{0x1.e16ee40000000p-4f,0x1.fc73780000000p-1f},{0x1.04abe80000000p-3f,0x1.fbd5f40000000p-1f},{0x1.1899ee0000000p-3f,0x1.fb2be80000000p-1f},{0x1.2c81060000000p-3f,0x1.fa75580000000p-1f},
{0x1.4060b60000000p-3f,0x1.f9b24a0000000p-1f},{0x1.54387e0000000p-3f,0x1.f8e2c00000000p-1f},{0x1.6807e20000000p-3f,0x1.f806c20000000p-1f},{0x1.7bce620000000p-3f,0x1.f71e540000000p-1f},
{0x1.8f8b840000000p-3f,0x1.f6297c0000000p-1f},{0x1.a33eca0000000p-3f,0x1.f528420000000p-1f},{0x1.b6e7b80000000p-3f,0x1.f41aa80000000p-1f},{0x1.ca85d20000000p-3f,0x1.f300b80000000p-1f},
{0x1.de189a0000000p-3f,0x1.f1da780000000p-1f},{0x1.f19f980000000p-3f,0x1.f0a7f00000000p-1f},{0x1.028d260000000p-2f,0x1.ef69260000000p-1f},{0x1.0c44200000000p-2f,0x1.ee1e220000000p-1f},
{0x1.15f47c0000000p-2f,0x1.ecc6ee0000000p-1f},{0x1.1f9dfc0000000p-2f,0x1.eb63920000000p-1f},{0x1.2940620000000p-2f,0x1.e9f4160000000p-1f},{0x1.32db740000000p-2f,0x1.e878820000000p-1f},
{0x1.3c6ef40000000p-2f,0x1.e6f0e20000000p-1f},{0x1.45faa40000000p-2f,0x1.e55d3c0000000p-1f},{0x1.4f7e4a0000000p-2f,0x1.e3bd9e0000000p-1f},{0x1.58f9a80000000p-2f,0x1.e212100000000p-1f},
{0x1.626c820000000p-2f,0x1.e05a9e0000000p-1f},{0x1.6bd69e0000000p-2f,0x1.de97500000000p-1f},{0x1.7537c20000000p-2f,0x1.dcc8340000000p-1f},{0x1.7e8fae0000000p-2f,0x1.daed540000000p-1f},
{0x1.87de2a0000000p-2f,0x1.d906bc0000000p-1f},{0x1.9122fc0000000p-2f,0x1.d7147a0000000p-1f},{0x1.9a5de60000000p-2f,0x1.d516960000000p-1f},{0x1.a38eb20000000p-2f,0x1.d30d200000000p-1f},
{0x1.acb5240000000p-2f,0x1.d0f8240000000p-1f},{0x1.b5d1000000000p-2f,0x1.ced7b00000000p-1f},{0x1.bee2100000000p-2f,0x1.ccabce0000000p-1f},{0x1.c7e81a0000000p-2f,0x1.ca74900000000p-1f},
{0x1.d0e2e20000000p-2f,0x1.c832020000000p-1f},{0x1.d9d2340000000p-2f,0x1.c5e4320000000p-1f},{0x1.e2b5d40000000p-2f,0x1.c38b300000000p-1f},{0x1.eb8d8a0000000p-2f,0x1.c127080000000p-1f},
{0x1.f459200000000p-2f,0x1.beb7cc0000000p-1f},{0x1.fd185e0000000p-2f,0x1.bc3d8a0000000p-1f},{0x1.02e5860000000p-1f,0x1.b9b8520000000p-1f},{0x1.07387a0000000p-1f,0x1.b728340000000p-1f},
{0x1.0b84ee0000000p-1f,0x1.b48d400000000p-1f},{0x1.0fcaca0000000p-1f,0x1.b1e7880000000p-1f},{0x1.1409f00000000p-1f,0x1.af37180000000p-1f},{0x1.1842480000000p-1f,0x1.ac7c060000000p-1f},
{0x1.1c73b40000000p-1f,0x1.a9b6620000000p-1f},{0x1.209e1c0000000p-1f,0x1.a6e63e0000000p-1f},{0x1.24c1640000000p-1f,0x1.a40ba80000000p-1f},{0x1.28dd740000000p-1f,0x1.a126b60000000p-1f},
{0x1.2cf2300000000p-1f,0x1.9e377a0000000p-1f},{0x1.30ff800000000p-1f,0x1.9b3e040000000p-1f},{0x1.3505480000000p-1f,0x1.983a6a0000000p-1f},{0x1.3903720000000p-1f,0x1.952cbc0000000p-1f},
{0x1.3cf9e20000000p-1f,0x1.9215100000000p-1f},{0x1.40e8800000000p-1f,0x1.8ef3780000000p-1f},{0x1.44cf320000000p-1f,0x1.8bc8060000000p-1f},{0x1.48ade20000000p-1f,0x1.8892d20000000p-1f},
{0x1.4c84740000000p-1f,0x1.8553ee0000000p-1f},{0x1.5052d40000000p-1f,0x1.820b700000000p-1f},{0x1.5418e60000000p-1f,0x1.7eb96a0000000p-1f},{0x1.57d6940000000p-1f,0x1.7b5df20000000p-1f},
{0x1.5b8bc60000000p-1f,0x1.77f91e0000000p-1f},{0x1.5f38640000000p-1f,0x1.748b040000000p-1f},{0x1.62dc580000000p-1f,0x1.7113b80000000p-1f},{0x1.66778c0000000p-1f,0x1.6d93520000000p-1f},
{0x1.6a09e60000000p-1f,0x1.6a09e60000000p-1f},{0x1.6d93520000000p-1f,0x1.66778c0000000p-1f},{0x1.7113b80000000p-1f,0x1.62dc580000000p-1f},{0x1.748b040000000p-1f,0x1.5f38640000000p-1f},
{0x1.77f91e0000000p-1f,0x1.5b8bc60000000p-1f},{0x1.7b5df20000000p-1f,0x1.57d6940000000p-1f},{0x1.7eb96a0000000p-1f,0x1.5418e60000000p-1f},{0x1.820b700000000p-1f,0x1.5052d40000000p-1f},
{0x1.8553ee0000000p-1f,0x1.4c84740000000p-1f},{0x1.8892d20000000p-1f,0x1.48ade20000000p-1f},{0x1.8bc8060000000p-1f,0x1.44cf320000000p-1f},{0x1.8ef3780000000p-1f,0x1.40e8800000000p-1f},
{0x1.9215100000000p-1f,0x1.3cf9e20000000p-1f},{0x1.952cbc0000000p-1f,0x1.3903720000000p-1f},{0x1.983a6a0000000p-1f,0x1.3505480000000p-1f},{0x1.9b3e040000000p-1f,0x1.30ff800000000p-1f},
{0x1.9e377a0000000p-1f,0x1.2cf2300000000p-1f},{0x1.a126b60000000p-1f,0x1.28dd740000000p-1f},{0x1.a40ba80000000p-1f,0x1.24c1640000000p-1f},{0x1.a6e63e0000000p-1f,0x1.209e1c0000000p-1f},
{0x1.a9b6620000000p-1f,0x1.1c73b40000000p-1f},{0x1.ac7c060000000p-1f,0x1.1842480000000p-1f},{0x1.af37180000000p-1f,0x1.1409f00000000p-1f},{0x1.b1e7880000000p-1f,0x1.0fcaca0000000p-1f},
{0x1.b48d400000000p-1f,0x1.0b84ee0000000p-1f},{0x1.b728340000000p-1f,0x1.07387a0000000p-1f},{0x1.b9b8520000000p-1f,0x1.02e5860000000p-1f},{0x1.bc3d8a0000000p-1f,0x1.fd185e0000000p-2f},
{0x1.beb7cc0000000p-1f,0x1.f459200000000p-2f},{0x1.c127080000000p-1f,0x1.eb8d8a0000000p-2f},{0x1.c38b300000000p-1f,0x1.e2b5d40000000p-2f},{0x1.c5e4320000000p-1f,0x1.d9d2340000000p-2f},
{0x1.c832020000000p-1f,0x1.d0e2e20000000p-2f},{0x1.ca74900000000p-1f,0x1.c7e81a0000000p-2f},{0x1.ccabce0000000p-1f,0x1.bee2100000000p-2f},{0x1.ced7b00000000p-1f,0x1.b5d1000000000p-2f},
{0x1.d0f8240000000p-1f,0x1.acb5240000000p-2f},{0x1.d30d200000000p-1f,0x1.a38eb20000000p-2f},{0x1.d516960000000p-1f,0x1.9a5de60000000p-2f},{0x1.d7147a0000000p-1f,0x1.9122fc0000000p-2f},
{0x1.d906bc0000000p-1f,0x1.87de2a0000000p-2f},{0x1.daed540000000p-1f,0x1.7e8fae0000000p-2f},{0x1.dcc8340000000p-1f,0x1.7537c20000000p-2f},{0x1.de97500000000p-1f,0x1.6bd69e0000000p-2f},
{0x1.e05a9e0000000p-1f,0x1.626c820000000p-2f},{0x1.e212100000000p-1f,0x1.58f9a80000000p-2f},{0x1.e3bd9e0000000p-1f,0x1.4f7e4a0000000p-2f},{0x1.e55d3c0000000p-1f,0x1.45faa40000000p-2f},
{0x1.e6f0e20000000p-1f,0x1.3c6ef40000000p-2f},{0x1.e878820000000p-1f,0x1.32db740000000p-2f},{0x1.e9f4160000000p-1f,0x1.2940620000000p-2f},{0x1.eb63920000000p-1f,0x1.1f9dfc0000000p-2f},
{0x1.ecc6ee0000000p-1f,0x1.15f47c0000000p-2f},{0x1.ee1e220000000p-1f,0x1.0c44200000000p-2f},{0x1.ef69260000000p-1f,0x1.028d260000000p-2f},{0x1.f0a7f00000000p-1f,0x1.f19f980000000p-3f},
{0x1.f1da780000000p-1f,0x1.de189a0000000p-3f},{0x1.f300b80000000p-1f,0x1.ca85d20000000p-3f},{0x1.f41aa80000000p-1f,0x1.b6e7b80000000p-3f},{0x1.f528420000000p-1f,0x1.a33eca0000000p-3f},
{0x1.f6297c0000000p-1f,0x1.8f8b840000000p-3f},{0x1.f71e540000000p-1f,0x1.7bce620000000p-3f},{0x1.f806c20000000p-1f,0x1.6807e20000000p-3f},{0x1.f8e2c00000000p-1f,0x1.54387e0000000p-3f},
{0x1.f9b24a0000000p-1f,0x1.4060b60000000p-3f},{0x1.fa75580000000p-1f,0x1.2c81060000000p-3f},{0x1.fb2be80000000p-1f,0x1.1899ee0000000p-3f},{0x1.fbd5f40000000p-1f,0x1.04abe80000000p-3f},
{0x1.fc73780000000p-1f,0x1.e16ee40000000p-4f},{0x1.fd04700000000p-1f,0x1.b97a1a0000000p-4f},{0x1.fd88da0000000p-1f,0x1.917a6c0000000p-4f},{0x1.fe00b20000000p-1f,0x1.6970d40000000p-4f},
{0x1.fe6bf20000000p-1f,0x1.415e540000000p-4f},{0x1.feca9c0000000p-1f,0x1.1943e40000000p-4f},{0x1.ff1cac0000000p-1f,0x1.e245060000000p-5f},{0x1.ff621e0000000p-1f,0x1.91f6600000000p-5f},
{0x1.ff9af20000000p-1f,0x1.419dce0000000p-5f},{0x1.ffc7280000000p-1f,0x1.e27a980000000p-6f},{0x1.ffe6bc0000000p-1f,0x1.41adac0000000p-6f},{0x1.fff9ae0000000p-1f,0x1.41b1a40000000p-7f}
};
__device__ const cfs TW2048_[2048] = {
{0x1.0000000000000p+0f,0x0.0p+0f},{0x1.ffff620000000p-1f,-0x1.921f8c0000000p-9f},{0x1.fffd880000000p-1f,-0x1.921f100000000p-8f},{0x1.fffa720000000p-1f,-0x1.2d96b00000000p-7f},
{0x1.fff6220000000p-1f,-0x1.921d200000000p-7f},{0x1.fff0940000000p-1f,-0x1.f6a2960000000p-7f},{0x1.ffe9cc0000000p-1f,-0x1.2d936c0000000p-6f},{0x1.ffe1c60000000p-1f,-0x1.5fd4d20000000p-6f},
{0x1.ffd8860000000p-1f,-0x1.9215600000000p-6f},{0x1.ffce0a0000000p-1f,-0x1.c454f40000000p-6f},{0x1.ffc2520000000p-1f,-0x1.f693740000000p-6f},{0x1.ffb55e0000000p-1f,-0x1.14685e0000000p-5f},
{0x1.ffa72e0000000p-1f,-0x1.2d86580000000p-5f},{0x1.ff97c40000000p-1f,-0x1.46a3960000000p-5f},{0x1.ff871e0000000p-1f,-0x1.5fc00e0000000p-5f},{0x1.ff753c0000000p-1f,-0x1.78dbaa0000000p-5f},
{0x1.ff621e0000000p-1f,-0x1.91f6600000000p-5f},{0x1.ff4dc60000000p-1f,-0x1.ab101c0000000p-5f},{0x1.ff38300000000p-1f,-0x1.c428d20000000p-5f},{0x1.ff21620000000p-1f,-0x1.dd40700000000p-5f},
{0x1.ff09560000000p-1f,-0x1.f656e80000000p-5f},{0x1.fef0100000000p-1f,-0x1.07b6140000000p-4f},{0x1.fed58e0000000p-1f,-0x1.1440140000000p-4f},{0x1.feb9d20000000p-1f,-0x1.20c9680000000p-4f},
{0x1.fe9cda0000000p-1f,-0x1.2d520a0000000p-4f},{0x1.fe7ea80000000p-1f,-0x1.39d9f20000000p-4f},{0x1.fe5f3a0000000p-1f,-0x1.4661180000000p-4f},{0x1.fe3e920000000p-1f,-0x1.52e7740000000p-4f},
{0x1.fe1cb00000000p-1f,-0x1.5f6d000000000p-4f},{0x1.fdf9920000000p-1f,-0x1.6bf1b40000000p-4f},{0x1.fdd53a0000000p-1f,-0x1.7875860000000p-4f},{0x1.fdafa80000000p-1f,-0x1.84f8720000000p-4f},
{0x1.fd88da0000000p-1f,-0x1.917a6c0000000p-4f},{0x1.fd60d20000000p-1f,-0x1.9dfb6e0000000p-4f},{0x1.fd37920000000p-1f,-0x1.aa7b720000000p-4f},{0x1.fd0d160000000p-1f,-0x1.b6fa6e0000000p-4f},
{0x1.fce1600000000p-1f,-0x1.c3785c0000000p-4f},{0x1.fcb4700000000p-1f,-0x1.cff5340000000p-4f},{0x1.fc86460000000p-1f,-0x1.dc70ec0000000p-4f},{0x1.fc56e40000000p-1f,-0x1.e8eb800000000p-4f},
{0x1.fc26480000000p-1f,-0x1.f564e60000000p-4f},{0x1.fbf4700000000p-1f,-0x1.00ee8a0000000p-3f},{0x1.fbc1620000000p-1f,-0x1.072a040000000p-3f},{0x1.fb8d180000000p-1f,-0x1.0d64dc0000000p-3f},
{0x1.fb57980000000p-1f,-0x1.139f0c0000000p-3f},{0x1.fb20dc0000000p-1f,-0x1.19d8940000000p-3f},{0x1.fae8e80000000p-1f,-0x1.20116e0000000p-3f},{0x1.faafbc0000000p-1f,-0x1.2649940000000p-3f},
{0x1.fa75580000000p-1f,-0x1.2c81060000000p-3f},{0x1.fa39ba0000000p-1f,-0x1.32b7c00000000p-3f},{0x1.f9fce60000000p-1f,-0x1.38edbc0000000p-3f},{0x1.f9bed80000000p-1f,-0x1.3f22f60000000p-3f},
{0x1.f97f920000000p-1f,-0x1.45576c0000000p-3f},{0x1.f93f140000000p-1f,-0x1.4b8b180000000p-3f},{0x1.f8fd600000000p-1f,-0x1.51bdf80000000p-3f},{0x1.f8ba740000000p-1f,-0x1.57f0080000000p-3f},
{0x1.f876500000000p-1f,-0x1.5e21440000000p-3f},{0x1.f830f40000000p-1f,-0x1.6451a80000000p-3f},{0x1.f7ea620000000p-1f,-0x1.6a81300000000p-3f},{0x1.f7a29a0000000p-1f,-0x1.70afd80000000p-3f},
{0x1.f7599a0000000p-1f,-0x1.76dd9e0000000p-3f},{0x1.f70f640000000p-1f,-0x1.7d0a7c0000000p-3f},{0x1.f6c3f80000000p-1f,-0x1.83366e0000000p-3f},{0x1.f677560000000p-1f,-0x1.8961720000000p-3f},
{0x1.f6297c0000000p-1f,-0x1.8f8b840000000p-3f},{0x1.f5da6e0000000p-1f,-0x1.95b49e0000000p-3f},{0x1.f58a2c0000000p-1f,-0x1.9bdcc00000000p-3f},{0x1.f538b20000000p-1f,-0x1.a203e20000000p-3f},
{0x1.f4e6040000000p-1f,-0x1.a82a020000000p-3f},{0x1.f492200000000p-1f,-0x1.ae4f1e0000000p-3f},{0x1.f43d080000000p-1f,-0x1.b4732e0000000p-3f},{0x1.f3e6bc0000000p-1f,-0x1.ba96340000000p-3f},
{0x1.f38f3a0000000p-1f,-0x1.c0b8260000000p-3f},{0x1.f336860000000p-1f,-0x1.c6d9060000000p-3f},{0x1.f2dc9c0000000p-1f,-0x1.ccf8cc0000000p-3f},{0x1.f281800000000p-1f,-0x1.d317740000000p-3f},
{0x1.f225300000000p-1f,-0x1.d934fe0000000p-3f},{0x1.f1c7ac0000000p-1f,-0x1.df51640000000p-3f},{0x1.f168f60000000p-1f,-0x1.e56ca20000000p-3f},{0x1.f1090c0000000p-1f,-0x1.eb86b40000000p-3f},
{0x1.f0a7f00000000p-1f,-0x1.f19f980000000p-3f},{0x1.f045a20000000p-1f,-0x1.f7b7480000000p-3f},{0x1.efe2200000000p-1f,-0x1.fdcdc20000000p-3f},{0x1.ef7d6e0000000p-1f,-0x1.01f1800000000p-2f},
{0x1.ef178a0000000p-1f,-0x1.04fb800000000p-2f},{0x1.eeb0740000000p-1f,-0x1.0804e00000000p-2f},{0x1.ee482e0000000p-1f,-0x1.0b0d9c0000000p-2f},{0x1.eddeb60000000p-1f,-0x1.0e15b40000000p-2f},
{0x1.ed740e0000000p-1f,-0x1.111d260000000p-2f},{0x1.ed08360000000p-1f,-0x1.1423ee0000000p-2f},{0x1.ec9b2e0000000p-1f,-0x1.172a0e0000000p-2f},{0x1.ec2cf40000000p-1f,-0x1.1a2f800000000p-2f},
{0x1.ebbd8c0000000p-1f,-0x1.1d34440000000p-2f},{0x1.eb4cf60000000p-1f,-0x1.2038580000000p-2f},{0x1.eadb2e0000000p-1f,-0x1.233bba0000000p-2f},{0x1.ea683a0000000p-1f,-0x1.263e6a0000000p-2f},
{0x1.e9f4160000000p-1f,-0x1.2940620000000p-2f},{0x1.e97ec40000000p-1f,-0x1.2c41a40000000p-2f},{0x1.e908440000000p-1f,-0x1.2f422e0000000p-2f},{0x1.e890960000000p-1f,-0x1.3241fc0000000p-2f},
{0x1.e817ba0000000p-1f,-0x1.35410c0000000p-2f},{0x1.e79db20000000p-1f,-0x1.383f5e0000000p-2f},{0x1.e7227e0000000p-1f,-0x1.3b3cf00000000p-2f},{0x1.e6a61c0000000p-1f,-0x1.3e39be0000000p-2f},
{0x1.e6288e0000000p-1f,-0x1.4135ca0000000p-2f},{0x1.e5a9d60000000p-1f,-0x1.44310e0000000p-2f},{0x1.e529f00000000p-1f,-0x1.472b8a0000000p-2f},{0x1.e4a8e00000000p-1f,-0x1.4a253e0000000p-2f},
{0x1.e426a40000000p-1f,-0x1.4d1e240000000p-2f},{0x1.e3a33e0000000p-1f,-0x1.50163e0000000p-2f},{0x1.e31eae0000000p-1f,-0x1.530d880000000p-2f},{0x1.e298f40000000p-1f,-0x1.5604020000000p-2f},
{0x1.e212100000000p-1f,-0x1.58f9a80000000p-2f},{0x1.e18a020000000p-1f,-0x1.5bee780000000p-2f},{0x1.e100cc0000000p-1f,-0x1.5ee2740000000p-2f},{0x1.e0766e0000000p-1f,-0x1.61d5960000000p-2f},
{0x1.dfeae60000000p-1f,-0x1.64c7de0000000p-2f},{0x1.df5e360000000p-1f,-0x1.67b94a0000000p-2f},{0x1.ded0600000000p-1f,-0x1.6aa9d80000000p-2f},{0x1.de41600000000p-1f,-0x1.6d99860000000p-2f},
{0x1.ddb13c0000000p-1f,-0x1.7088540000000p-2f},{0x1.dd1ff00000000p-1f,-0x1.73763c0000000p-2f},{0x1.dc8d7c0000000p-1f,-0x1.7663400000000p-2f},{0x1.dbf9e40000000p-1f,-0x1.794f5e0000000p-2f},
{0x1.db65260000000p-1f,-0x1.7c3a940000000p-2f},{0x1.dacf420000000p-1f,-0x1.7f24de0000000p-2f},{0x1.da383a0000000p-1f,-0x1.820e3c0000000p-2f},{0x1.d9a00e0000000p-1f,-0x1.84f6aa0000000p-2f},
{0x1.d906bc0000000p-1f,-0x1.87de2a0000000p-2f},{0x1.d86c480000000p-1f,-0x1.8ac4b80000000p-2f},{0x1.d7d0b00000000p-1f,-0x1.8daa520000000p-2f},{0x1.d733f60000000p-1f,-0x1.908ef80000000p-2f},
{0x1.d696180000000p-1f,-0x1.9372a60000000p-2f},{0x1.d5f7180000000p-1f,-0x1.96555c0000000p-2f},{0x1.d556f60000000p-1f,-0x1.9937160000000p-2f},{0x1.d4b5b20000000p-1f,-0x1.9c17d40000000p-2f},
{0x1.d4134e0000000p-1f,-0x1.9ef7940000000p-2f},{0x1.d36fc80000000p-1f,-0x1.a1d6540000000p-2f},{0x1.d2cb220000000p-1f,-0x1.a4b4120000000p-2f},{0x1.d2255c0000000p-1f,-0x1.a790ce0000000p-2f},
{0x1.d17e780000000p-1f,-0x1.aa6c820000000p-2f},{0x1.d0d6720000000p-1f,-0x1.ad47320000000p-2f},{0x1.d02d500000000p-1f,-0x1.b020d60000000p-2f},{0x1.cf830e0000000p-1f,-0x1.b2f9720000000p-2f},
{0x1.ced7b00000000p-1f,-0x1.b5d1000000000p-2f},{0x1.ce2b320000000p-1f,-0x1.b8a7820000000p-2f},{0x1.cd7d980000000p-1f,-0x1.bb7cf20000000p-2f},{0x1.cccee20000000p-1f,-0x1.be51520000000p-2f},
{0x1.cc1f100000000p-1f,-0x1.c1249e0000000p-2f},{0x1.cb6e200000000p-1f,-0x1.c3f6d40000000p-2f},{0x1.cabc160000000p-1f,-0x1.c6c7f40000000p-2f},{0x1.ca08f20000000p-1f,-0x1.c997fc0000000p-2f},
{0x1.c954b20000000p-1f,-0x1.cc66ea0000000p-2f},{0x1.c89f580000000p-1f,-0x1.cf34ba0000000p-2f},{0x1.c7e8e60000000p-1f,-0x1.d2016e0000000p-2f},{0x1.c731580000000p-1f,-0x1.d4cd020000000p-2f},
{0x1.c678b40000000p-1f,-0x1.d797760000000p-2f},{0x1.c5bef60000000p-1f,-0x1.da60c60000000p-2f},{0x1.c504200000000p-1f,-0x1.dd28f20000000p-2f},{0x1.c448340000000p-1f,-0x1.dfeff60000000p-2f},
{0x1.c38b300000000p-1f,-0x1.e2b5d40000000p-2f},{0x1.c2cd140000000p-1f,-0x1.e57a860000000p-2f},{0x1.c20de40000000p-1f,-0x1.e83e0e0000000p-2f},{0x1.c14d9e0000000p-1f,-0x1.eb006a0000000p-2f},
{0x1.c08c420000000p-1f,-0x1.edc1960000000p-2f},{0x1.bfc9d20000000p-1f,-0x1.f081900000000p-2f},{0x1.bf064e0000000p-1f,-0x1.f3405a0000000p-2f},{0x1.be41b60000000p-1f,-0x1.f5fdee0000000p-2f},
{0x1.bd7c0a0000000p-1f,-0x1.f8ba4e0000000p-2f},{0x1.bcb54c0000000p-1f,-0x1.fb75760000000p-2f},{0x1.bbed7c0000000p-1f,-0x1.fe2f640000000p-2f},{0x1.bb249a0000000p-1f,-0x1.00740c0000000p-1f},
{0x1.ba5aa60000000p-1f,-0x1.01cfc80000000p-1f},{0x1.b98fa20000000p-1f,-0x1.032ae60000000p-1f},{0x1.b8c38e0000000p-1f,-0x1.0485620000000p-1f},{0x1.b7f6680000000p-1f,-0x1.05df3e0000000p-1f},
{0x1.b728340000000p-1f,-0x1.07387a0000000p-1f},{0x1.b658f20000000p-1f,-0x1.0891120000000p-1f},{0x1.b588a00000000p-1f,-0x1.09e9080000000p-1f},{0x1.b4b7400000000p-1f,-0x1.0b40580000000p-1f},
{0x1.b3e4d40000000p-1f,-0x1.0c97040000000p-1f},{0x1.b3115a0000000p-1f,-0x1.0ded0c0000000p-1f},{0x1.b23cd40000000p-1f,-0x1.0f426c0000000p-1f},{0x1.b167420000000p-1f,-0x1.1097240000000p-1f},
{0x1.b090a60000000p-1f,-0x1.11eb360000000p-1f},{0x1.afb8fe0000000p-1f,-0x1.133e9c0000000p-1f},{0x1.aee04c0000000p-1f,-0x1.14915a0000000p-1f},{0x1.ae06900000000p-1f,-0x1.15e36e0000000p-1f},
{0x1.ad2bca0000000p-1f,-0x1.1734d60000000p-1f},{0x1.ac4ffc0000000p-1f,-0x1.1885920000000p-1f},{0x1.ab73260000000p-1f,-0x1.19d5a00000000p-1f},{0x1.aa95480000000p-1f,-0x1.1b25020000000p-1f},
{0x1.a9b6620000000p-1f,-0x1.1c73b40000000p-1f},{0x1.a8d6760000000p-1f,-0x1.1dc1b60000000p-1f},{0x1.a7f5860000000p-1f,-0x1.1f0f080000000p-1f},{0x1.a7138e0000000p-1f,-0x1.205baa0000000p-1f},
{0x1.a630920000000p-1f,-0x1.21a79a0000000p-1f},{0x1.a54c920000000p-1f,-0x1.22f2d60000000p-1f},{0x1.a4678c0000000p-1f,-0x1.243d600000000p-1f},{0x1.a381840000000p-1f,-0x1.2587340000000p-1f},
{0x1.a29a7a0000000p-1f,-0x1.26d0540000000p-1f},{0x1.a1b26e0000000p-1f,-0x1.2818be0000000p-1f},{0x1.a0c95e0000000p-1f,-0x1.2960720000000p-1f},{0x1.9fdf500000000p-1f,-0x1.2aa76e0000000p-1f},
{0x1.9ef43e0000000p-1f,-0x1.2bedb20000000p-1f},{0x1.9e082e0000000p-1f,-0x1.2d333e0000000p-1f},{0x1.9d1b200000000p-1f,-0x1.2e780e0000000p-1f},{0x1.9c2d120000000p-1f,-0x1.2fbc240000000p-1f},
{0x1.9b3e040000000p-1f,-0x1.30ff800000000p-1f},{0x1.9a4dfa0000000p-1f,-0x1.32421e0000000p-1f},{0x1.995cf20000000p-1f,-0x1.3384000000000p-1f},{0x1.986af00000000p-1f,-0x1.34c5260000000p-1f},
{0x1.9777f00000000p-1f,-0x1.36058c0000000p-1f},{0x1.9683f40000000p-1f,-0x1.3745320000000p-1f},{0x1.958efe0000000p-1f,-0x1.3884180000000p-1f},{0x1.94990e0000000p-1f,-0x1.39c23e0000000p-1f},
{0x1.93a2240000000p-1f,-0x1.3affa20000000p-1f},{0x1.92aa420000000p-1f,-0x1.3c3c440000000p-1f},{0x1.91b1660000000p-1f,-0x1.3d78240000000p-1f},{0x1.90b7940000000p-1f,-0x1.3eb33e0000000p-1f},
{0x1.8fbcca0000000p-1f,-0x1.3fed960000000p-1f},{0x1.8ec10a0000000p-1f,-0x1.4127260000000p-1f},{0x1.8dc4540000000p-1f,-0x1.425ff20000000p-1f},{0x1.8cc6a80000000p-1f,-0x1.4397f60000000p-1f},
{0x1.8bc8060000000p-1f,-0x1.44cf320000000p-1f},{0x1.8ac8720000000p-1f,-0x1.4605a60000000p-1f},{0x1.89c7ea0000000p-1f,-0x1.473b520000000p-1f},{0x1.88c66e0000000p-1f,-0x1.4870340000000p-1f},
{0x1.87c4000000000p-1f,-0x1.49a44a0000000p-1f},{0x1.86c0a20000000p-1f,-0x1.4ad7960000000p-1f},{0x1.85bc520000000p-1f,-0x1.4c0a140000000p-1f},{0x1.84b7120000000p-1f,-0x1.4d3bc60000000p-1f},
{0x1.83b0e00000000p-1f,-0x1.4e6cac0000000p-1f},{0x1.82a9c20000000p-1f,-0x1.4f9cc20000000p-1f},{0x1.81a1b40000000p-1f,-0x1.50cc0a0000000p-1f},{0x1.8098b80000000p-1f,-0x1.51fa820000000p-1f},
{0x1.7f8ece0000000p-1f,-0x1.53282a0000000p-1f},{0x1.7e83f80000000p-1f,-0x1.5455000000000p-1f},{0x1.7d78360000000p-1f,-0x1.5581040000000p-1f},{0x1.7c6b8a0000000p-1f,-0x1.56ac360000000p-1f},
{0x1.7b5df20000000p-1f,-0x1.57d6940000000p-1f},{0x1.7a4f700000000p-1f,-0x1.59001e0000000p-1f},{0x1.7940060000000p-1f,-0x1.5a28d20000000p-1f},{0x1.782fb20000000p-1f,-0x1.5b50b20000000p-1f},
{0x1.771e760000000p-1f,-0x1.5c77bc0000000p-1f},{0x1.760c520000000p-1f,-0x1.5d9dee0000000p-1f},{0x1.74f9480000000p-1f,-0x1.5ec34a0000000p-1f},{0x1.73e5580000000p-1f,-0x1.5fe7cc0000000p-1f},
{0x1.72d0840000000p-1f,-0x1.610b760000000p-1f},{0x1.71baca0000000p-1f,-0x1.622e440000000p-1f},{0x1.70a42c0000000p-1f,-0x1.63503a0000000p-1f},{0x1.6f8caa0000000p-1f,-0x1.6471540000000p-1f},
{0x1.6e74460000000p-1f,-0x1.6591920000000p-1f},{0x1.6d5afe0000000p-1f,-0x1.66b0f40000000p-1f},{0x1.6c40d80000000p-1f,-0x1.67cf780000000p-1f},{0x1.6b25ce0000000p-1f,-0x1.68ed1e0000000p-1f},
{0x1.6a09e60000000p-1f,-0x1.6a09e60000000p-1f},{0x1.68ed1e0000000p-1f,-0x1.6b25ce0000000p-1f},{0x1.67cf780000000p-1f,-0x1.6c40d80000000p-1f},{0x1.66b0f40000000p-1f,-0x1.6d5afe0000000p-1f},
{0x1.6591920000000p-1f,-0x1.6e74460000000p-1f},{0x1.6471540000000p-1f,-0x1.6f8caa0000000p-1f},{0x1.63503a0000000p-1f,-0x1.70a42c0000000p-1f},{0x1.622e440000000p-1f,-0x1.71baca0000000p-1f},
{0x1.610b760000000p-1f,-0x1.72d0840000000p-1f},{0x1.5fe7cc0000000p-1f,-0x1.73e5580000000p-1f},{0x1.5ec34a0000000p-1f,-0x1.74f9480000000p-1f},{0x1.5d9dee0000000p-1f,-0x1.760c520000000p-1f},
{0x1.5c77bc0000000p-1f,-0x1.771e760000000p-1f},{0x1.5b50b20000000p-1f,-0x1.782fb20000000p-1f},{0x1.5a28d20000000p-1f,-0x1.7940060000000p-1f},{0x1.59001e0000000p-1f,-0x1.7a4f700000000p-1f},
{0x1.57d6940000000p-1f,-0x1.7b5df20000000p-1f},{0x1.56ac360000000p-1f,-0x1.7c6b8a0000000p-1f},{0x1.5581040000000p-1f,-0x1.7d78360000000p-1f},{0x1.5455000000000p-1f,-0x1.7e83f80000000p-1f},
{0x1.53282a0000000p-1f,-0x1.7f8ece0000000p-1f},{0x1.51fa820000000p-1f,-0x1.8098b80000000p-1f},{0x1.50cc0a0000000p-1f,-0x1.81a1b40000000p-1f},{0x1.4f9cc20000000p-1f,-0x1.82a9c20000000p-1f},
{0x1.4e6cac0000000p-1f,-0x1.83b0e00000000p-1f},{0x1.4d3bc60000000p-1f,-0x1.84b7120000000p-1f},{0x1.4c0a140000000p-1f,-0x1.85bc520000000p-1f},{0x1.4ad7960000000p-1f,-0x1.86c0a20000000p-1f},
{0x1.49a44a0000000p-1f,-0x1.87c4000000000p-1f},{0x1.4870340000000p-1f,-0x1.88c66e0000000p-1f},{0x1.473b520000000p-1f,-0x1.89c7ea0000000p-1f},{0x1.4605a60000000p-1f,-0x1.8ac8720000000p-1f},
{0x1.44cf320000000p-1f,-0x1.8bc8060000000p-1f},{0x1.4397f60000000p-1f,-0x1.8cc6a80000000p-1f},{0x1.425ff20000000p-1f,-0x1.8dc4540000000p-1f},{0x1.4127260000000p-1f,-0x1.8ec10a0000000p-1f},
{0x1.3fed960000000p-1f,-0x1.8fbcca0000000p-1f},{0x1.3eb33e0000000p-1f,-0x1.90b7940000000p-1f},{0x1.3d78240000000p-1f,-0x1.91b1660000000p-1f},{0x1.3c3c440000000p-1f,-0x1.92aa420000000p-1f},
{0x1.3affa20000000p-1f,-0x1.93a2240000000p-1f},{0x1.39c23e0000000p-1f,-0x1.94990e0000000p-1f},{0x1.3884180000000p-1f,-0x1.958efe0000000p-1f},{0x1.3745320000000p-1f,-0x1.9683f40000000p-1f},
{0x1.36058c0000000p-1f,-0x1.9777f00000000p-1f},{0x1.34c5260000000p-1f,-0x1.986af00000000p-1f},{0x1.3384000000000p-1f,-0x1.995cf20000000p-1f},{0x1.32421e0000000p-1f,-0x1.9a4dfa0000000p-1f},
{0x1.30ff800000000p-1f,-0x1.9b3e040000000p-1f},{0x1.2fbc240000000p-1f,-0x1.9c2d120000000p-1f},{0x1.2e780e0000000p-1f,-0x1.9d1b200000000p-1f},{0x1.2d333e0000000p-1f,-0x1.9e082e0000000p-1f},
{0x1.2bedb20000000p-1f,-0x1.9ef43e0000000p-1f},{0x1.2aa76e0000000p-1f,-0x1.9fdf500000000p-1f},{0x1.2960720000000p-1f,-0x1.a0c95e0000000p-1f},{0x1.2818be0000000p-1f,-0x1.a1b26e0000000p-1f},
{0x1.26d0540000000p-1f,-0x1.a29a7a0000000p-1f},{0x1.2587340000000p-1f,-0x1.a381840000000p-1f},{0x1.243d600000000p-1f,-0x1.a4678c0000000p-1f},{0x1.22f2d60000000p-1f,-0x1.a54c920000000p-1f},
{0x1.21a79a0000000p-1f,-0x1.a630920000000p-1f},{0x1.205baa0000000p-1f,-0x1.a7138e0000000p-1f},{0x1.1f0f080000000p-1f,-0x1.a7f5860000000p-1f},{0x1.1dc1b60000000p-1f,-0x1.a8d6760000000p-1f},
{0x1.1c73b40000000p-1f,-0x1.a9b6620000000p-1f},{0x1.1b25020000000p-1f,-0x1.aa95480000000p-1f},{0x1.19d5a00000000p-1f,-0x1.ab73260000000p-1f},{0x1.1885920000000p-1f,-0x1.ac4ffc0000000p-1f},
{0x1.1734d60000000p-1f,-0x1.ad2bca0000000p-1f},{0x1.15e36e0000000p-1f,-0x1.ae06900000000p-1f},{0x1.14915a0000000p-1f,-0x1.aee04c0000000p-1f},{0x1.133e9c0000000p-1f,-0x1.afb8fe0000000p-1f},
{0x1.11eb360000000p-1f,-0x1.b090a60000000p-1f},{0x1.1097240000000p-1f,-0x1.b167420000000p-1f},{0x1.0f426c0000000p-1f,-0x1.b23cd40000000p-1f},{0x1.0ded0c0000000p-1f,-0x1.b3115a0000000p-1f},
{0x1.0c97040000000p-1f,-0x1.b3e4d40000000p-1f},{0x1.0b40580000000p-1f,-0x1.b4b7400000000p-1f},{0x1.09e9080000000p-1f,-0x1.b588a00000000p-1f},{0x1.0891120000000p-1f,-0x1.b658f20000000p-1f},
{0x1.07387a0000000p-1f,-0x1.b728340000000p-1f},{0x1.05df3e0000000p-1f,-0x1.b7f6680000000p-1f},{0x1.0485620000000p-1f,-0x1.b8c38e0000000p-1f},{0x1.032ae60000000p-1f,-0x1.b98fa20000000p-1f},
{0x1.01cfc80000000p-1f,-0x1.ba5aa60000000p-1f},{0x1.00740c0000000p-1f,-0x1.bb249a0000000p-1f},{0x1.fe2f640000000p-2f,-0x1.bbed7c0000000p-1f},{0x1.fb75760000000p-2f,-0x1.bcb54c0000000p-1f},
{0x1.f8ba4e0000000p-2f,-0x1.bd7c0a0000000p-1f},{0x1.f5fdee0000000p-2f,-0x1.be41b60000000p-1f},{0x1.f3405a0000000p-2f,-0x1.bf064e0000000p-1f},{0x1.f081900000000p-2f,-0x1.bfc9d20000000p-1f},
{0x1.edc1960000000p-2f,-0x1.c08c420000000p-1f},{0x1.eb006a0000000p-2f,-0x1.c14d9e0000000p-1f},{0x1.e83e0e0000000p-2f,-0x1.c20de40000000p-1f},{0x1.e57a860000000p-2f,-0x1.c2cd140000000p-1f},
{0x1.e2b5d40000000p-2f,-0x1.c38b300000000p-1f},{0x1.dfeff60000000p-2f,-0x1.c448340000000p-1f},{0x1.dd28f20000000p-2f,-0x1.c504200000000p-1f},{0x1.da60c60000000p-2f,-0x1.c5bef60000000p-1f},
{0x1.d797760000000p-2f,-0x1.c678b40000000p-1f},{0x1.d4cd020000000p-2f,-0x1.c731580000000p-1f},{0x1.d2016e0000000p-2f,-0x1.c7e8e60000000p-1f},{0x1.cf34ba0000000p-2f,-0x1.c89f580000000p-1f},
{0x1.cc66ea0000000p-2f,-0x1.c954b20000000p-1f},{0x1.c997fc0000000p-2f,-0x1.ca08f20000000p-1f},{0x1.c6c7f40000000p-2f,-0x1.cabc160000000p-1f},{0x1.c3f6d40000000p-2f,-0x1.cb6e200000000p-1f},
{0x1.c1249e0000000p-2f,-0x1.cc1f100000000p-1f},{0x1.be51520000000p-2f,-0x1.cccee20000000p-1f},{0x1.bb7cf20000000p-2f,-0x1.cd7d980000000p-1f},{0x1.b8a7820000000p-2f,-0x1.ce2b320000000p-1f},
{0x1.b5d1000000000p-2f,-0x1.ced7b00000000p-1f},{0x1.b2f9720000000p-2f,-0x1.cf830e0000000p-1f},{0x1.b020d60000000p-2f,-0x1.d02d500000000p-1f},{0x1.ad47320000000p-2f,-0x1.d0d6720000000p-1f},
{0x1.aa6c820000000p-2f,-0x1.d17e780000000p-1f},{0x1.a790ce0000000p-2f,-0x1.d2255c0000000p-1f},{0x1.a4b4120000000p-2f,-0x1.d2cb220000000p-1f},{0x1.a1d6540000000p-2f,-0x1.d36fc80000000p-1f},
{0x1.9ef7940000000p-2f,-0x1.d4134e0000000p-1f},{0x1.9c17d40000000p-2f,-0x1.d4b5b20000000p-1f},{0x1.9937160000000p-2f,-0x1.d556f60000000p-1f},{0x1.96555c0000000p-2f,-0x1.d5f7180000000p-1f},
{0x1.9372a60000000p-2f,-0x1.d696180000000p-1f},{0x1.908ef80000000p-2f,-0x1.d733f60000000p-1f},{0x1.8daa520000000p-2f,-0x1.d7d0b00000000p-1f},{0x1.8ac4b80000000p-2f,-0x1.d86c480000000p-1f},
{0x1.87de2a0000000p-2f,-0x1.d906bc0000000p-1f},{0x1.84f6aa0000000p-2f,-0x1.d9a00e0000000p-1f},{0x1.820e3c0000000p-2f,-0x1.da383a0000000p-1f},{0x1.7f24de0000000p-2f,-0x1.dacf420000000p-1f},
{0x1.7c3a940000000p-2f,-0x1.db65260000000p-1f},{0x1.794f5e0000000p-2f,-0x1.dbf9e40000000p-1f},{0x1.7663400000000p-2f,-0x1.dc8d7c0000000p-1f},{0x1.73763c0000000p-2f,-0x1.dd1ff00000000p-1f},
{0x1.7088540000000p-2f,-0x1.ddb13c0000000p-1f},{0x1.6d99860000000p-2f,-0x1.de41600000000p-1f},{0x1.6aa9d80000000p-2f,-0x1.ded0600000000p-1f},{0x1.67b94a0000000p-2f,-0x1.df5e360000000p-1f},
{0x1.64c7de0000000p-2f,-0x1.dfeae60000000p-1f},{0x1.61d5960000000p-2f,-0x1.e0766e0000000p-1f},{0x1.5ee2740000000p-2f,-0x1.e100cc0000000p-1f},{0x1.5bee780000000p-2f,-0x1.e18a020000000p-1f},
{0x1.58f9a80000000p-2f,-0x1.e212100000000p-1f},{0x1.5604020000000p-2f,-0x1.e298f40000000p-1f},{0x1.530d880000000p-2f,-0x1.e31eae0000000p-1f},{0x1.50163e0000000p-2f,-0x1.e3a33e0000000p-1f},
{0x1.4d1e240000000p-2f,-0x1.e426a40000000p-1f},{0x1.4a253e0000000p-2f,-0x1.e4a8e00000000p-1f},{0x1.472b8a0000000p-2f,-0x1.e529f00000000p-1f},{0x1.44310e0000000p-2f,-0x1.e5a9d60000000p-1f},
{0x1.4135ca0000000p-2f,-0x1.e6288e0000000p-1f},{0x1.3e39be0000000p-2f,-0x1.e6a61c0000000p-1f},{0x1.3b3cf00000000p-2f,-0x1.e7227e0000000p-1f},{0x1.383f5e0000000p-2f,-0x1.e79db20000000p-1f},
{0x1.35410c0000000p-2f,-0x1.e817ba0000000p-1f},{0x1.3241fc0000000p-2f,-0x1.e890960000000p-1f},{0x1.2f422e0000000p-2f,-0x1.e908440000000p-1f},{0x1.2c41a40000000p-2f,-0x1.e97ec40000000p-1f},
{0x1.2940620000000p-2f,-0x1.e9f4160000000p-1f},{0x1.263e6a0000000p-2f,-0x1.ea683a0000000p-1f},{0x1.233bba0000000p-2f,-0x1.eadb2e0000000p-1f},{0x1.2038580000000p-2f,-0x1.eb4cf60000000p-1f},
{0x1.1d34440000000p-2f,-0x1.ebbd8c0000000p-1f},{0x1.1a2f800000000p-2f,-0x1.ec2cf40000000p-1f},{0x1.172a0e0000000p-2f,-0x1.ec9b2e0000000p-1f},{0x1.1423ee0000000p-2f,-0x1.ed08360000000p-1f},
{0x1.111d260000000p-2f,-0x1.ed740e0000000p-1f},{0x1.0e15b40000000p-2f,-0x1.eddeb60000000p-1f},{0x1.0b0d9c0000000p-2f,-0x1.ee482e0000000p-1f},{0x1.0804e00000000p-2f,-0x1.eeb0740000000p-1f},
{0x1.04fb800000000p-2f,-0x1.ef178a0000000p-1f},{0x1.01f1800000000p-2f,-0x1.ef7d6e0000000p-1f},{0x1.fdcdc20000000p-3f,-0x1.efe2200000000p-1f},{0x1.f7b7480000000p-3f,-0x1.f045a20000000p-1f},
{0x1.f19f980000000p-3f,-0x1.f0a7f00000000p-1f},{0x1.eb86b40000000p-3f,-0x1.f1090c0000000p-1f},{0x1.e56ca20000000p-3f,-0x1.f168f60000000p-1f},{0x1.df51640000000p-3f,-0x1.f1c7ac0000000p-1f},
{0x1.d934fe0000000p-3f,-0x1.f225300000000p-1f},{0x1.d317740000000p-3f,-0x1.f281800000000p-1f},{0x1.ccf8cc0000000p-3f,-0x1.f2dc9c0000000p-1f},{0x1.c6d9060000000p-3f,-0x1.f336860000000p-1f},
{0x1.c0b8260000000p-3f,-0x1.f38f3a0000000p-1f},{0x1.ba96340000000p-3f,-0x1.f3e6bc0000000p-1f},{0x1.b4732e0000000p-3f,-0x1.f43d080000000p-1f},{0x1.ae4f1e0000000p-3f,-0x1.f492200000000p-1f},
{0x1.a82a020000000p-3f,-0x1.f4e6040000000p-1f},{0x1.a203e20000000p-3f,-0x1.f538b20000000p-1f},{0x1.9bdcc00000000p-3f,-0x1.f58a2c0000000p-1f},{0x1.95b49e0000000p-3f,-0x1.f5da6e0000000p-1f},
{0x1.8f8b840000000p-3f,-0x1.f6297c0000000p-1f},{0x1.8961720000000p-3f,-0x1.f677560000000p-1f},{0x1.83366e0000000p-3f,-0x1.f6c3f80000000p-1f},{0x1.7d0a7c0000000p-3f,-0x1.f70f640000000p-1f},
{0x1.76dd9e0000000p-3f,-0x1.f7599a0000000p-1f},{0x1.70afd80000000p-3f,-0x1.f7a29a0000000p-1f},{0x1.6a81300000000p-3f,-0x1.f7ea620000000p-1f},{0x1.6451a80000000p-3f,-0x1.f830f40000000p-1f},
{0x1.5e21440000000p-3f,-0x1.f876500000000p-1f},{0x1.57f0080000000p-3f,-0x1.f8ba740000000p-1f},{0x1.51bdf80000000p-3f,-0x1.f8fd600000000p-1f},{0x1.4b8b180000000p-3f,-0x1.f93f140000000p-1f},
{0x1.45576c0000000p-3f,-0x1.f97f920000000p-1f},{0x1.3f22f60000000p-3f,-0x1.f9bed80000000p-1f},{0x1.38edbc0000000p-3f,-0x1.f9fce60000000p-1f},{0x1.32b7c00000000p-3f,-0x1.fa39ba0000000p-1f},
{0x1.2c81060000000p-3f,-0x1.fa75580000000p-1f},{0x1.2649940000000p-3f,-0x1.faafbc0000000p-1f},{0x1.20116e0000000p-3f,-0x1.fae8e80000000p-1f},{0x1.19d8940000000p-3f,-0x1.fb20dc0000000p-1f},
{0x1.139f0c0000000p-3f,-0x1.fb57980000000p-1f},{0x1.0d64dc0000000p-3f,-0x1.fb8d180000000p-1f},{0x1.072a040000000p-3f,-0x1.fbc1620000000p-1f},{0x1.00ee8a0000000p-3f,-0x1.fbf4700000000p-1f},
{0x1.f564e60000000p-4f,-0x1.fc26480000000p-1f},{0x1.e8eb800000000p-4f,-0x1.fc56e40000000p-1f},{0x1.dc70ec0000000p-4f,-0x1.fc86460000000p-1f},{0x1.cff5340000000p-4f,-0x1.fcb4700000000p-1f},
{0x1.c3785c0000000p-4f,-0x1.fce1600000000p-1f},{0x1.b6fa6e0000000p-4f,-0x1.fd0d160000000p-1f},{0x1.aa7b720000000p-4f,-0x1.fd37920000000p-1f},{0x1.9dfb6e0000000p-4f,-0x1.fd60d20000000p-1f},
{0x1.917a6c0000000p-4f,-0x1.fd88da0000000p-1f},{0x1.84f8720000000p-4f,-0x1.fdafa80000000p-1f},{0x1.7875860000000p-4f,-0x1.fdd53a0000000p-1f},{0x1.6bf1b40000000p-4f,-0x1.fdf9920000000p-1f},
{0x1.5f6d000000000p-4f,-0x1.fe1cb00000000p-1f},{0x1.52e7740000000p-4f,-0x1.fe3e920000000p-1f},{0x1.4661180000000p-4f,-0x1.fe5f3a0000000p-1f},{0x1.39d9f20000000p-4f,-0x1.fe7ea80000000p-1f},
{0x1.2d520a0000000p-4f,-0x1.fe9cda0000000p-1f},{0x1.20c9680000000p-4f,-0x1.feb9d20000000p-1f},{0x1.1440140000000p-4f,-0x1.fed58e0000000p-1f},{0x1.07b6140000000p-4f,-0x1.fef0100000000p-1f},
{0x1.f656e80000000p-5f,-0x1.ff09560000000p-1f},{0x1.dd40700000000p-5f,-0x1.ff21620000000p-1f},{0x1.c428d20000000p-5f,-0x1.ff38300000000p-1f},{0x1.ab101c0000000p-5f,-0x1.ff4dc60000000p-1f},
{0x1.91f6600000000p-5f,-0x1.ff621e0000000p-1f},{0x1.78dbaa0000000p-5f,-0x1.ff753c0000000p-1f},{0x1.5fc00e0000000p-5f,-0x1.ff871e0000000p-1f},{0x1.46a3960000000p-5f,-0x1.ff97c40000000p-1f},
{0x1.2d86580000000p-5f,-0x1.ffa72e0000000p-1f},{0x1.14685e0000000p-5f,-0x1.ffb55e0000000p-1f},{0x1.f693740000000p-6f,-0x1.ffc2520000000p-1f},{0x1.c454f40000000p-6f,-0x1.ffce0a0000000p-1f},
{0x1.9215600000000p-6f,-0x1.ffd8860000000p-1f},{0x1.5fd4d20000000p-6f,-0x1.ffe1c60000000p-1f},{0x1.2d936c0000000p-6f,-0x1.ffe9cc0000000p-1f},{0x1.f6a2960000000p-7f,-0x1.fff0940000000p-1f},
{0x1.921d200000000p-7f,-0x1.fff6220000000p-1f},{0x1.2d96b00000000p-7f,-0x1.fffa720000000p-1f},{0x1.921f100000000p-8f,-0x1.fffd880000000p-1f},{0x1.921f8c0000000p-9f,-0x1.ffff620000000p-1f},
{0x0.0p+0f,-0x1.0000000000000p+0f},{-0x1.921f8c0000000p-9f,-0x1.ffff620000000p-1f},{-0x1.921f100000000p-8f,-0x1.fffd880000000p-1f},{-0x1.2d96b00000000p-7f,-0x1.fffa720000000p-1f},
{-0x1.921d200000000p-7f,-0x1.fff6220000000p-1f},{-0x1.f6a2960000000p-7f,-0x1.fff0940000000p-1f},{-0x1.2d936c0000000p-6f,-0x1.ffe9cc0000000p-1f},{-0x1.5fd4d20000000p-6f,-0x1.ffe1c60000000p-1f},
{-0x1.9215600000000p-6f,-0x1.ffd8860000000p-1f},{-0x1.c454f40000000p-6f,-0x1.ffce0a0000000p-1f},{-0x1.f693740000000p-6f,-0x1.ffc2520000000p-1f},{-0x1.14685e0000000p-5f,-0x1.ffb55e0000000p-1f},
{-0x1.2d86580000000p-5f,-0x1.ffa72e0000000p-1f},{-0x1.46a3960000000p-5f,-0x1.ff97c40000000p-1f},{-0x1.5fc00e0000000p-5f,-0x1.ff871e0000000p-1f},{-0x1.78dbaa0000000p-5f,-0x1.ff753c0000000p-1f},
{-0x1.91f6600000000p-5f,-0x1.ff621e0000000p-1f},{-0x1.ab101c0000000p-5f,-0x1.ff4dc60000000p-1f},{-0x1.c428d20000000p-5f,-0x1.ff38300000000p-1f},{-0x1.dd40700000000p-5f,-0x1.ff21620000000p-1f},
{-0x1.f656e80000000p-5f,-0x1.ff09560000000p-1f},{-0x1.07b6140000000p-4f,-0x1.fef0100000000p-1f},{-0x1.1440140000000p-4f,-0x1.fed58e0000000p-1f},{-0x1.20c9680000000p-4f,-0x1.feb9d20000000p-1f},
{-0x1.2d520a0000000p-4f,-0x1.fe9cda0000000p-1f},{-0x1.39d9f20000000p-4f,-0x1.fe7ea80000000p-1f},{-0x1.4661180000000p-4f,-0x1.fe5f3a0000000p-1f},{-0x1.52e7740000000p-4f,-0x1.fe3e920000000p-1f},
{-0x1.5f6d000000000p-4f,-0x1.fe1cb00000000p-1f},{-0x1.6bf1b40000000p-4f,-0x1.fdf9920000000p-1f},{-0x1.7875860000000p-4f,-0x1.fdd53a0000000p-1f},{-0x1.84f8720000000p-4f,-0x1.fdafa80000000p-1f},
{-0x1.917a6c0000000p-4f,-0x1.fd88da0000000p-1f},{-0x1.9dfb6e0000000p-4f,-0x1.fd60d20000000p-1f},{-0x1.aa7b720000000p-4f,-0x1.fd37920000000p-1f},{-0x1.b6fa6e0000000p-4f,-0x1.fd0d160000000p-1f},
{-0x1.c3785c0000000p-4f,-0x1.fce1600000000p-1f},{-0x1.cff5340000000p-4f,-0x1.fcb4700000000p-1f},{-0x1.dc70ec0000000p-4f,-0x1.fc86460000000p-1f},{-0x1.e8eb800000000p-4f,-0x1.fc56e40000000p-1f},
{-0x1.f564e60000000p-4f,-0x1.fc26480000000p-1f},{-0x1.00ee8a0000000p-3f,-0x1.fbf4700000000p-1f},{-0x1.072a040000000p-3f,-0x1.fbc1620000000p-1f},{-0x1.0d64dc0000000p-3f,-0x1.fb8d180000000p-1f},
{-0x1.139f0c0000000p-3f,-0x1.fb57980000000p-1f},{-0x1.19d8940000000p-3f,-0x1.fb20dc0000000p-1f},{-0x1.20116e0000000p-3f,-0x1.fae8e80000000p-1f},{-0x1.2649940000000p-3f,-0x1.faafbc0000000p-1f},
{-0x1.2c81060000000p-3f,-0x1.fa75580000000p-1f},{-0x1.32b7c00000000p-3f,-0x1.fa39ba0000000p-1f},{-0x1.38edbc0000000p-3f,-0x1.f9fce60000000p-1f},{-0x1.3f22f60000000p-3f,-0x1.f9bed80000000p-1f},
{-0x1.45576c0000000p-3f,-0x1.f97f920000000p-1f},{-0x1.4b8b180000000p-3f,-0x1.f93f140000000p-1f},{-0x1.51bdf80000000p-3f,-0x1.f8fd600000000p-1f},{-0x1.57f0080000000p-3f,-0x1.f8ba740000000p-1f},
{-0x1.5e21440000000p-3f,-0x1.f876500000000p-1f},{-0x1.6451a80000000p-3f,-0x1.f830f40000000p-1f},{-0x1.6a81300000000p-3f,-0x1.f7ea620000000p-1f},{-0x1.70afd80000000p-3f,-0x1.f7a29a0000000p-1f},
{-0x1.76dd9e0000000p-3f,-0x1.f7599a0000000p-1f},{-0x1.7d0a7c0000000p-3f,-0x1.f70f640000000p-1f},{-0x1.83366e0000000p-3f,-0x1.f6c3f80000000p-1f},{-0x1.8961720000000p-3f,-0x1.f677560000000p-1f},
{-0x1.8f8b840000000p-3f,-0x1.f6297c0000000p-1f},{-0x1.95b49e0000000p-3f,-0x1.f5da6e0000000p-1f},{-0x1.9bdcc00000000p-3f,-0x1.f58a2c0000000p-1f},{-0x1.a203e20000000p-3f,-0x1.f538b20000000p-1f},
{-0x1.a82a020000000p-3f,-0x1.f4e6040000000p-1f},{-0x1.ae4f1e0000000p-3f,-0x1.f492200000000p-1f},{-0x1.b4732e0000000p-3f,-0x1.f43d080000000p-1f},{-0x1.ba96340000000p-3f,-0x1.f3e6bc0000000p-1f},
{-0x1.c0b8260000000p-3f,-0x1.f38f3a0000000p-1f},{-0x1.c6d9060000000p-3f,-0x1.f336860000000p-1f},{-0x1.ccf8cc0000000p-3f,-0x1.f2dc9c0000000p-1f},{-0x1.d317740000000p-3f,-0x1.f281800000000p-1f},
{-0x1.d934fe0000000p-3f,-0x1.f225300000000p-1f},{-0x1.df51640000000p-3f,-0x1.f1c7ac0000000p-1f},{-0x1.e56ca20000000p-3f,-0x1.f168f60000000p-1f},{-0x1.eb86b40000000p-3f,-0x1.f1090c0000000p-1f},
{-0x1.f19f980000000p-3f,-0x1.f0a7f00000000p-1f},{-0x1.f7b7480000000p-3f,-0x1.f045a20000000p-1f},{-0x1.fdcdc20000000p-3f,-0x1.efe2200000000p-1f},{-0x1.01f1800000000p-2f,-0x1.ef7d6e0000000p-1f},
{-0x1.04fb800000000p-2f,-0x1.ef178a0000000p-1f},{-0x1.0804e00000000p-2f,-0x1.eeb0740000000p-1f},{-0x1.0b0d9c0000000p-2f,-0x1.ee482e0000000p-1f},{-0x1.0e15b40000000p-2f,-0x1.eddeb60000000p-1f},
{-0x1.111d260000000p-2f,-0x1.ed740e0000000p-1f},{-0x1.1423ee0000000p-2f,-0x1.ed08360000000p-1f},{-0x1.172a0e0000000p-2f,-0x1.ec9b2e0000000p-1f},{-0x1.1a2f800000000p-2f,-0x1.ec2cf40000000p-1f},
{-0x1.1d34440000000p-2f,-0x1.ebbd8c0000000p-1f},{-0x1.2038580000000p-2f,-0x1.eb4cf60000000p-1f},{-0x1.233bba0000000p-2f,-0x1.eadb2e0000000p-1f},{-0x1.263e6a0000000p-2f,-0x1.ea683a0000000p-1f},
{-0x1.2940620000000p-2f,-0x1.e9f4160000000p-1f},{-0x1.2c41a40000000p-2f,-0x1.e97ec40000000p-1f},{-0x1.2f422e0000000p-2f,-0x1.e908440000000p-1f},{-0x1.3241fc0000000p-2f,-0x1.e890960000000p-1f},
{-0x1.35410c0000000p-2f,-0x1.e817ba0000000p-1f},{-0x1.383f5e0000000p-2f,-0x1.e79db20000000p-1f},{-0x1.3b3cf00000000p-2f,-0x1.e7227e0000000p-1f},{-0x1.3e39be0000000p-2f,-0x1.e6a61c0000000p-1f},
{-0x1.4135ca0000000p-2f,-0x1.e6288e0000000p-1f},{-0x1.44310e0000000p-2f,-0x1.e5a9d60000000p-1f},{-0x1.472b8a0000000p-2f,-0x1.e529f00000000p-1f},{-0x1.4a253e0000000p-2f,-0x1.e4a8e00000000p-1f},
{-0x1.4d1e240000000p-2f,-0x1.e426a40000000p-1f},{-0x1.50163e0000000p-2f,-0x1.e3a33e0000000p-1f},{-0x1.530d880000000p-2f,-0x1.e31eae0000000p-1f},{-0x1.5604020000000p-2f,-0x1.e298f40000000p-1f},
{-0x1.58f9a80000000p-2f,-0x1.e212100000000p-1f},{-0x1.5bee780000000p-2f,-0x1.e18a020000000p-1f},{-0x1.5ee2740000000p-2f,-0x1.e100cc0000000p-1f},{-0x1.61d5960000000p-2f,-0x1.e0766e0000000p-1f},
{-0x1.64c7de0000000p-2f,-0x1.dfeae60000000p-1f},{-0x1.67b94a0000000p-2f,-0x1.df5e360000000p-1f},{-0x1.6aa9d80000000p-2f,-0x1.ded0600000000p-1f},{-0x1.6d99860000000p-2f,-0x1.de41600000000p-1f},
{-0x1.7088540000000p-2f,-0x1.ddb13c0000000p-1f},{-0x1.73763c0000000p-2f,-0x1.dd1ff00000000p-1f},{-0x1.7663400000000p-2f,-0x1.dc8d7c0000000p-1f},{-0x1.794f5e0000000p-2f,-0x1.dbf9e40000000p-1f},
{-0x1.7c3a940000000p-2f,-0x1.db65260000000p-1f},{-0x1.7f24de0000000p-2f,-0x1.dacf420000000p-1f},{-0x1.820e3c0000000p-2f,-0x1.da383a0000000p-1f},{-0x1.84f6aa0000000p-2f,-0x1.d9a00e0000000p-1f},
{-0x1.87de2a0000000p-2f,-0x1.d906bc0000000p-1f},{-0x1.8ac4b80000000p-2f,-0x1.d86c480000000p-1f},{-0x1.8daa520000000p-2f,-0x1.d7d0b00000000p-1f},{-0x1.908ef80000000p-2f,-0x1.d733f60000000p-1f},
{-0x1.9372a60000000p-2f,-0x1.d696180000000p-1f},{-0x1.96555c0000000p-2f,-0x1.d5f7180000000p-1f},{-0x1.9937160000000p-2f,-0x1.d556f60000000p-1f},{-0x1.9c17d40000000p-2f,-0x1.d4b5b20000000p-1f},
{-0x1.9ef7940000000p-2f,-0x1.d4134e0000000p-1f},{-0x1.a1d6540000000p-2f,-0x1.d36fc80000000p-1f},{-0x1.a4b4120000000p-2f,-0x1.d2cb220000000p-1f},{-0x1.a790ce0000000p-2f,-0x1.d2255c0000000p-1f},
{-0x1.aa6c820000000p-2f,-0x1.d17e780000000p-1f},{-0x1.ad47320000000p-2f,-0x1.d0d6720000000p-1f},{-0x1.b020d60000000p-2f,-0x1.d02d500000000p-1f},{-0x1.b2f9720000000p-2f,-0x1.cf830e0000000p-1f},
{-0x1.b5d1000000000p-2f,-0x1.ced7b00000000p-1f},{-0x1.b8a7820000000p-2f,-0x1.ce2b320000000p-1f},{-0x1.bb7cf20000000p-2f,-0x1.cd7d980000000p-1f},{-0x1.be51520000000p-2f,-0x1.cccee20000000p-1f},
{-0x1.c1249e0000000p-2f,-0x1.cc1f100000000p-1f},{-0x1.c3f6d40000000p-2f,-0x1.cb6e200000000p-1f},{-0x1.c6c7f40000000p-2f,-0x1.cabc160000000p-1f},{-0x1.c997fc0000000p-2f,-0x1.ca08f20000000p-1f},
{-0x1.cc66ea0000000p-2f,-0x1.c954b20000000p-1f},{-0x1.cf34ba0000000p-2f,-0x1.c89f580000000p-1f},{-0x1.d2016e0000000p-2f,-0x1.c7e8e60000000p-1f},{-0x1.d4cd020000000p-2f,-0x1.c731580000000p-1f},
{-0x1.d797760000000p-2f,-0x1.c678b40000000p-1f},{-0x1.da60c60000000p-2f,-0x1.c5bef60000000p-1f},{-0x1.dd28f20000000p-2f,-0x1.c504200000000p-1f},{-0x1.dfeff60000000p-2f,-0x1.c448340000000p-1f},
{-0x1.e2b5d40000000p-2f,-0x1.c38b300000000p-1f},{-0x1.e57a860000000p-2f,-0x1.c2cd140000000p-1f},{-0x1.e83e0e0000000p-2f,-0x1.c20de40000000p-1f},{-0x1.eb006a0000000p-2f,-0x1.c14d9e0000000p-1f},
{-0x1.edc1960000000p-2f,-0x1.c08c420000000p-1f},{-0x1.f081900000000p-2f,-0x1.bfc9d20000000p-1f},{-0x1.f3405a0000000p-2f,-0x1.bf064e0000000p-1f},{-0x1.f5fdee0000000p-2f,-0x1.be41b60000000p-1f},
{-0x1.f8ba4e0000000p-2f,-0x1.bd7c0a0000000p-1f},{-0x1.fb75760000000p-2f,-0x1.bcb54c0000000p-1f},{-0x1.fe2f640000000p-2f,-0x1.bbed7c0000000p-1f},{-0x1.00740c0000000p-1f,-0x1.bb249a0000000p-1f},
{-0x1.01cfc80000000p-1f,-0x1.ba5aa60000000p-1f},{-0x1.032ae60000000p-1f,-0x1.b98fa20000000p-1f},{-0x1.0485620000000p-1f,-0x1.b8c38e0000000p-1f},{-0x1.05df3e0000000p-1f,-0x1.b7f6680000000p-1f},
{-0x1.07387a0000000p-1f,-0x1.b728340000000p-1f},{-0x1.0891120000000p-1f,-0x1.b658f20000000p-1f},{-0x1.09e9080000000p-1f,-0x1.b588a00000000p-1f},{-0x1.0b40580000000p-1f,-0x1.b4b7400000000p-1f},
{-0x1.0c97040000000p-1f,-0x1.b3e4d40000000p-1f},{-0x1.0ded0c0000000p-1f,-0x1.b3115a0000000p-1f},{-0x1.0f426c0000000p-1f,-0x1.b23cd40000000p-1f},{-0x1.1097240000000p-1f,-0x1.b167420000000p-1f},
{-0x1.11eb360000000p-1f,-0x1.b090a60000000p-1f},{-0x1.133e9c0000000p-1f,-0x1.afb8fe0000000p-1f},{-0x1.14915a0000000p-1f,-0x1.aee04c0000000p-1f},{-0x1.15e36e0000000p-1f,-0x1.ae06900000000p-1f},
{-0x1.1734d60000000p-1f,-0x1.ad2bca0000000p-1f},{-0x1.1885920000000p-1f,-0x1.ac4ffc0000000p-1f},{-0x1.19d5a00000000p-1f,-0x1.ab73260000000p-1f},{-0x1.1b25020000000p-1f,-0x1.aa95480000000p-1f},
{-0x1.1c73b40000000p-1f,-0x1.a9b6620000000p-1f},{-0x1.1dc1b60000000p-1f,-0x1.a8d6760000000p-1f},{-0x1.1f0f080000000p-1f,-0x1.a7f5860000000p-1f},{-0x1.205baa0000000p-1f,-0x1.a7138e0000000p-1f},
{-0x1.21a79a0000000p-1f,-0x1.a630920000000p-1f},{-0x1.22f2d60000000p-1f,-0x1.a54c920000000p-1f},{-0x1.243d600000000p-1f,-0x1.a4678c0000000p-1f},{-0x1.2587340000000p-1f,-0x1.a381840000000p-1f},
{-0x1.26d0540000000p-1f,-0x1.a29a7a0000000p-1f},{-0x1.2818be0000000p-1f,-0x1.a1b26e0000000p-1f},{-0x1.2960720000000p-1f,-0x1.a0c95e0000000p-1f},{-0x1.2aa76e0000000p-1f,-0x1.9fdf500000000p-1f},
{-0x1.2bedb20000000p-1f,-0x1.9ef43e0000000p-1f},{-0x1.2d333e0000000p-1f,-0x1.9e082e0000000p-1f},{-0x1.2e780e0000000p-1f,-0x1.9d1b200000000p-1f},{-0x1.2fbc240000000p-1f,-0x1.9c2d120000000p-1f},
{-0x1.30ff800000000p-1f,-0x1.9b3e040000000p-1f},{-0x1.32421e0000000p-1f,-0x1.9a4dfa0000000p-1f},{-0x1.3384000000000p-1f,-0x1.995cf20000000p-1f},{-0x1.34c5260000000p-1f,-0x1.986af00000000p-1f},
{-0x1.36058c0000000p-1f,-0x1.9777f00000000p-1f},{-0x1.3745320000000p-1f,-0x1.9683f40000000p-1f},{-0x1.3884180000000p-1f,-0x1.958efe0000000p-1f},{-0x1.39c23e0000000p-1f,-0x1.94990e0000000p-1f},
{-0x1.3affa20000000p-1f,-0x1.93a2240000000p-1f},{-0x1.3c3c440000000p-1f,-0x1.92aa420000000p-1f},{-0x1.3d78240000000p-1f,-0x1.91b1660000000p-1f},{-0x1.3eb33e0000000p-1f,-0x1.90b7940000000p-1f},
{-0x1.3fed960000000p-1f,-0x1.8fbcca0000000p-1f},{-0x1.4127260000000p-1f,-0x1.8ec10a0000000p-1f},{-0x1.425ff20000000p-1f,-0x1.8dc4540000000p-1f},{-0x1.4397f60000000p-1f,-0x1.8cc6a80000000p-1f},
{-0x1.44cf320000000p-1f,-0x1.8bc8060000000p-1f},{-0x1.4605a60000000p-1f,-0x1.8ac8720000000p-1f},{-0x1.473b520000000p-1f,-0x1.89c7ea0000000p-1f},{-0x1.4870340000000p-1f,-0x1.88c66e0000000p-1f},
{-0x1.49a44a0000000p-1f,-0x1.87c4000000000p-1f},{-0x1.4ad7960000000p-1f,-0x1.86c0a20000000p-1f},{-0x1.4c0a140000000p-1f,-0x1.85bc520000000p-1f},{-0x1.4d3bc60000000p-1f,-0x1.84b7120000000p-1f},
{-0x1.4e6cac0000000p-1f,-0x1.83b0e00000000p-1f},{-0x1.4f9cc20000000p-1f,-0x1.82a9c20000000p-1f},{-0x1.50cc0a0000000p-1f,-0x1.81a1b40000000p-1f},{-0x1.51fa820000000p-1f,-0x1.8098b80000000p-1f},
{-0x1.53282a0000000p-1f,-0x1.7f8ece0000000p-1f},{-0x1.5455000000000p-1f,-0x1.7e83f80000000p-1f},{-0x1.5581040000000p-1f,-0x1.7d78360000000p-1f},{-0x1.56ac360000000p-1f,-0x1.7c6b8a0000000p-1f},
{-0x1.57d6940000000p-1f,-0x1.7b5df20000000p-1f},{-0x1.59001e0000000p-1f,-0x1.7a4f700000000p-1f},{-0x1.5a28d20000000p-1f,-0x1.7940060000000p-1f},{-0x1.5b50b20000000p-1f,-0x1.782fb20000000p-1f},
{-0x1.5c77bc0000000p-1f,-0x1.771e760000000p-1f},{-0x1.5d9dee0000000p-1f,-0x1.760c520000000p-1f},{-0x1.5ec34a0000000p-1f,-0x1.74f9480000000p-1f},{-0x1.5fe7cc0000000p-1f,-0x1.73e5580000000p-1f},
{-0x1.610b760000000p-1f,-0x1.72d0840000000p-1f},{-0x1.622e440000000p-1f,-0x1.71baca0000000p-1f},{-0x1.63503a0000000p-1f,-0x1.70a42c0000000p-1f},{-0x1.6471540000000p-1f,-0x1.6f8caa0000000p-1f},
{-0x1.6591920000000p-1f,-0x1.6e74460000000p-1f},{-0x1.66b0f40000000p-1f,-0x1.6d5afe0000000p-1f},{-0x1.67cf780000000p-1f,-0x1.6c40d80000000p-1f},{-0x1.68ed1e0000000p-1f,-0x1.6b25ce0000000p-1f},
{-0x1.6a09e60000000p-1f,-0x1.6a09e60000000p-1f},{-0x1.6b25ce0000000p-1f,-0x1.68ed1e0000000p-1f},{-0x1.6c40d80000000p-1f,-0x1.67cf780000000p-1f},{-0x1.6d5afe0000000p-1f,-0x1.66b0f40000000p-1f},
{-0x1.6e74460000000p-1f,-0x1.6591920000000p-1f},{-0x1.6f8caa0000000p-1f,-0x1.6471540000000p-1f},{-0x1.70a42c0000000p-1f,-0x1.63503a0000000p-1f},{-0x1.71baca0000000p-1f,-0x1.622e440000000p-1f},
{-0x1.72d0840000000p-1f,-0x1.610b760000000p-1f},{-0x1.73e5580000000p-1f,-0x1.5fe7cc0000000p-1f},{-0x1.74f9480000000p-1f,-0x1.5ec34a0000000p-1f},{-0x1.760c520000000p-1f,-0x1.5d9dee0000000p-1f},
{-0x1.771e760000000p-1f,-0x1.5c77bc0000000p-1f},{-0x1.782fb20000000p-1f,-0x1.5b50b20000000p-1f},{-0x1.7940060000000p-1f,-0x1.5a28d20000000p-1f},{-0x1.7a4f700000000p-1f,-0x1.59001e0000000p-1f},
{-0x1.7b5df20000000p-1f,-0x1.57d6940000000p-1f},{-0x1.7c6b8a0000000p-1f,-0x1.56ac360000000p-1f},{-0x1.7d78360000000p-1f,-0x1.5581040000000p-1f},{-0x1.7e83f80000000p-1f,-0x1.5455000000000p-1f},
{-0x1.7f8ece0000000p-1f,-0x1.53282a0000000p-1f},{-0x1.8098b80000000p-1f,-0x1.51fa820000000p-1f},{-0x1.81a1b40000000p-1f,-0x1.50cc0a0000000p-1f},{-0x1.82a9c20000000p-1f,-0x1.4f9cc20000000p-1f},
{-0x1.83b0e00000000p-1f,-0x1.4e6cac0000000p-1f},{-0x1.84b7120000000p-1f,-0x1.4d3bc60000000p-1f},{-0x1.85bc520000000p-1f,-0x1.4c0a140000000p-1f},{-0x1.86c0a20000000p-1f,-0x1.4ad7960000000p-1f},
{-0x1.87c4000000000p-1f,-0x1.49a44a0000000p-1f},{-0x1.88c66e0000000p-1f,-0x1.4870340000000p-1f},{-0x1.89c7ea0000000p-1f,-0x1.473b520000000p-1f},{-0x1.8ac8720000000p-1f,-0x1.4605a60000000p-1f},
{-0x1.8bc8060000000p-1f,-0x1.44cf320000000p-1f},{-0x1.8cc6a80000000p-1f,-0x1.4397f60000000p-1f},{-0x1.8dc4540000000p-1f,-0x1.425ff20000000p-1f},{-0x1.8ec10a0000000p-1f,-0x1.4127260000000p-1f},
{-0x1.8fbcca0000000p-1f,-0x1.3fed960000000p-1f},{-0x1.90b7940000000p-1f,-0x1.3eb33e0000000p-1f},{-0x1.91b1660000000p-1f,-0x1.3d78240000000p-1f},{-0x1.92aa420000000p-1f,-0x1.3c3c440000000p-1f},
{-0x1.93a2240000000p-1f,-0x1.3affa20000000p-1f},{-0x1.94990e0000000p-1f,-0x1.39c23e0000000p-1f},{-0x1.958efe0000000p-1f,-0x1.3884180000000p-1f},{-0x1.9683f40000000p-1f,-0x1.3745320000000p-1f},
{-0x1.9777f00000000p-1f,-0x1.36058c0000000p-1f},{-0x1.986af00000000p-1f,-0x1.34c5260000000p-1f},{-0x1.995cf20000000p-1f,-0x1.3384000000000p-1f},{-0x1.9a4dfa0000000p-1f,-0x1.32421e0000000p-1f},
{-0x1.9b3e040000000p-1f,-0x1.30ff800000000p-1f},{-0x1.9c2d120000000p-1f,-0x1.2fbc240000000p-1f},{-0x1.9d1b200000000p-1f,-0x1.2e780e0000000p-1f},{-0x1.9e082e0000000p-1f,-0x1.2d333e0000000p-1f},
{-0x1.9ef43e0000000p-1f,-0x1.2bedb20000000p-1f},{-0x1.9fdf500000000p-1f,-0x1.2aa76e0000000p-1f},{-0x1.a0c95e0000000p-1f,-0x1.2960720000000p-1f},{-0x1.a1b26e0000000p-1f,-0x1.2818be0000000p-1f},
{-0x1.a29a7a0000000p-1f,-0x1.26d0540000000p-1f},{-0x1.a381840000000p-1f,-0x1.2587340000000p-1f},{-0x1.a4678c0000000p-1f,-0x1.243d600000000p-1f},{-0x1.a54c920000000p-1f,-0x1.22f2d60000000p-1f},
{-0x1.a630920000000p-1f,-0x1.21a79a0000000p-1f},{-0x1.a7138e0000000p-1f,-0x1.205baa0000000p-1f},{-0x1.a7f5860000000p-1f,-0x1.1f0f080000000p-1f},{-0x1.a8d6760000000p-1f,-0x1.1dc1b60000000p-1f},
{-0x1.a9b6620000000p-1f,-0x1.1c73b40000000p-1f},{-0x1.aa95480000000p-1f,-0x1.1b25020000000p-1f},{-0x1.ab73260000000p-1f,-0x1.19d5a00000000p-1f},{-0x1.ac4ffc0000000p-1f,-0x1.1885920000000p-1f},
{-0x1.ad2bca0000000p-1f,-0x1.1734d60000000p-1f},{-0x1.ae06900000000p-1f,-0x1.15e36e0000000p-1f},{-0x1.aee04c0000000p-1f,-0x1.14915a0000000p-1f},{-0x1.afb8fe0000000p-1f,-0x1.133e9c0000000p-1f},
{-0x1.b090a60000000p-1f,-0x1.11eb360000000p-1f},{-0x1.b167420000000p-1f,-0x1.1097240000000p-1f},{-0x1.b23cd40000000p-1f,-0x1.0f426c0000000p-1f},{-0x1.b3115a0000000p-1f,-0x1.0ded0c0000000p-1f},
{-0x1.b3e4d40000000p-1f,-0x1.0c97040000000p-1f},{-0x1.b4b7400000000p-1f,-0x1.0b40580000000p-1f},{-0x1.b588a00000000p-1f,-0x1.09e9080000000p-1f},{-0x1.b658f20000000p-1f,-0x1.0891120000000p-1f},
{-0x1.b728340000000p-1f,-0x1.07387a0000000p-1f},{-0x1.b7f6680000000p-1f,-0x1.05df3e0000000p-1f},{-0x1.b8c38e0000000p-1f,-0x1.0485620000000p-1f},{-0x1.b98fa20000000p-1f,-0x1.032ae60000000p-1f},
{-0x1.ba5aa60000000p-1f,-0x1.01cfc80000000p-1f},{-0x1.bb249a0000000p-1f,-0x1.00740c0000000p-1f},{-0x1.bbed7c0000000p-1f,-0x1.fe2f640000000p-2f},{-0x1.bcb54c0000000p-1f,-0x1.fb75760000000p-2f},
{-0x1.bd7c0a0000000p-1f,-0x1.f8ba4e0000000p-2f},{-0x1.be41b60000000p-1f,-0x1.f5fdee0000000p-2f},{-0x1.bf064e0000000p-1f,-0x1.f3405a0000000p-2f},{-0x1.bfc9d20000000p-1f,-0x1.f081900000000p-2f},
{-0x1.c08c420000000p-1f,-0x1.edc1960000000p-2f},{-0x1.c14d9e0000000p-1f,-0x1.eb006a0000000p-2f},{-0x1.c20de40000000p-1f,-0x1.e83e0e0000000p-2f},{-0x1.c2cd140000000p-1f,-0x1.e57a860000000p-2f},
{-0x1.c38b300000000p-1f,-0x1.e2b5d40000000p-2f},{-0x1.c448340000000p-1f,-0x1.dfeff60000000p-2f},{-0x1.c504200000000p-1f,-0x1.dd28f20000000p-2f},{-0x1.c5bef60000000p-1f,-0x1.da60c60000000p-2f},
{-0x1.c678b40000000p-1f,-0x1.d797760000000p-2f},{-0x1.c731580000000p-1f,-0x1.d4cd020000000p-2f},{-0x1.c7e8e60000000p-1f,-0x1.d2016e0000000p-2f},{-0x1.c89f580000000p-1f,-0x1.cf34ba0000000p-2f},
{-0x1.c954b20000000p-1f,-0x1.cc66ea0000000p-2f},{-0x1.ca08f20000000p-1f,-0x1.c997fc0000000p-2f},{-0x1.cabc160000000p-1f,-0x1.c6c7f40000000p-2f},{-0x1.cb6e200000000p-1f,-0x1.c3f6d40000000p-2f},
{-0x1.cc1f100000000p-1f,-0x1.c1249e0000000p-2f},{-0x1.cccee20000000p-1f,-0x1.be51520000000p-2f},{-0x1.cd7d980000000p-1f,-0x1.bb7cf20000000p-2f},{-0x1.ce2b320000000p-1f,-0x1.b8a7820000000p-2f},
{-0x1.ced7b00000000p-1f,-0x1.b5d1000000000p-2f},{-0x1.cf830e0000000p-1f,-0x1.b2f9720000000p-2f},{-0x1.d02d500000000p-1f,-0x1.b020d60000000p-2f},{-0x1.d0d6720000000p-1f,-0x1.ad47320000000p-2f},
{-0x1.d17e780000000p-1f,-0x1.aa6c820000000p-2f},{-0x1.d2255c0000000p-1f,-0x1.a790ce0000000p-2f},{-0x1.d2cb220000000p-1f,-0x1.a4b4120000000p-2f},{-0x1.d36fc80000000p-1f,-0x1.a1d6540000000p-2f},
{-0x1.d4134e0000000p-1f,-0x1.9ef7940000000p-2f},{-0x1.d4b5b20000000p-1f,-0x1.9c17d40000000p-2f},{-0x1.d556f60000000p-1f,-0x1.9937160000000p-2f},{-0x1.d5f7180000000p-1f,-0x1.96555c0000000p-2f},
{-0x1.d696180000000p-1f,-0x1.9372a60000000p-2f},{-0x1.d733f60000000p-1f,-0x1.908ef80000000p-2f},{-0x1.d7d0b00000000p-1f,-0x1.8daa520000000p-2f},{-0x1.d86c480000000p-1f,-0x1.8ac4b80000000p-2f},
{-0x1.d906bc0000000p-1f,-0x1.87de2a0000000p-2f},{-0x1.d9a00e0000000p-1f,-0x1.84f6aa0000000p-2f},{-0x1.da383a0000000p-1f,-0x1.820e3c0000000p-2f},{-0x1.dacf420000000p-1f,-0x1.7f24de0000000p-2f},
{-0x1.db65260000000p-1f,-0x1.7c3a940000000p-2f},{-0x1.dbf9e40000000p-1f,-0x1.794f5e0000000p-2f},{-0x1.dc8d7c0000000p-1f,-0x1.7663400000000p-2f},{-0x1.dd1ff00000000p-1f,-0x1.73763c0000000p-2f},
{-0x1.ddb13c0000000p-1f,-0x1.7088540000000p-2f},{-0x1.de41600000000p-1f,-0x1.6d99860000000p-2f},{-0x1.ded0600000000p-1f,-0x1.6aa9d80000000p-2f},{-0x1.df5e360000000p-1f,-0x1.67b94a0000000p-2f},
{-0x1.dfeae60000000p-1f,-0x1.64c7de0000000p-2f},{-0x1.e0766e0000000p-1f,-0x1.61d5960000000p-2f},{-0x1.e100cc0000000p-1f,-0x1.5ee2740000000p-2f},{-0x1.e18a020000000p-1f,-0x1.5bee780000000p-2f},
{-0x1.e212100000000p-1f,-0x1.58f9a80000000p-2f},{-0x1.e298f40000000p-1f,-0x1.5604020000000p-2f},{-0x1.e31eae0000000p-1f,-0x1.530d880000000p-2f},{-0x1.e3a33e0000000p-1f,-0x1.50163e0000000p-2f},
{-0x1.e426a40000000p-1f,-0x1.4d1e240000000p-2f},{-0x1.e4a8e00000000p-1f,-0x1.4a253e0000000p-2f},{-0x1.e529f00000000p-1f,-0x1.472b8a0000000p-2f},{-0x1.e5a9d60000000p-1f,-0x1.44310e0000000p-2f},
{-0x1.e6288e0000000p-1f,-0x1.4135ca0000000p-2f},{-0x1.e6a61c0000000p-1f,-0x1.3e39be0000000p-2f},{-0x1.e7227e0000000p-1f,-0x1.3b3cf00000000p-2f},{-0x1.e79db20000000p-1f,-0x1.383f5e0000000p-2f},
{-0x1.e817ba0000000p-1f,-0x1.35410c0000000p-2f},{-0x1.e890960000000p-1f,-0x1.3241fc0000000p-2f},{-0x1.e908440000000p-1f,-0x1.2f422e0000000p-2f},{-0x1.e97ec40000000p-1f,-0x1.2c41a40000000p-2f},
{-0x1.e9f4160000000p-1f,-0x1.2940620000000p-2f},{-0x1.ea683a0000000p-1f,-0x1.263e6a0000000p-2f},{-0x1.eadb2e0000000p-1f,-0x1.233bba0000000p-2f},{-0x1.eb4cf60000000p-1f,-0x1.2038580000000p-2f},
{-0x1.ebbd8c0000000p-1f,-0x1.1d34440000000p-2f},{-0x1.ec2cf40000000p-1f,-0x1.1a2f800000000p-2f},{-0x1.ec9b2e0000000p-1f,-0x1.172a0e0000000p-2f},{-0x1.ed08360000000p-1f,-0x1.1423ee0000000p-2f},
{-0x1.ed740e0000000p-1f,-0x1.111d260000000p-2f},{-0x1.eddeb60000000p-1f,-0x1.0e15b40000000p-2f},{-0x1.ee482e0000000p-1f,-0x1.0b0d9c0000000p-2f},{-0x1.eeb0740000000p-1f,-0x1.0804e00000000p-2f},
{-0x1.ef178a0000000p-1f,-0x1.04fb800000000p-2f},{-0x1.ef7d6e0000000p-1f,-0x1.01f1800000000p-2f},{-0x1.efe2200000000p-1f,-0x1.fdcdc20000000p-3f},{-0x1.f045a20000000p-1f,-0x1.f7b7480000000p-3f},
{-0x1.f0a7f00000000p-1f,-0x1.f19f980000000p-3f},{-0x1.f1090c0000000p-1f,-0x1.eb86b40000000p-3f},{-0x1.f168f60000000p-1f,-0x1.e56ca20000000p-3f},{-0x1.f1c7ac0000000p-1f,-0x1.df51640000000p-3f},
{-0x1.f225300000000p-1f,-0x1.d934fe0000000p-3f},{-0x1.f281800000000p-1f,-0x1.d317740000000p-3f},{-0x1.f2dc9c0000000p-1f,-0x1.ccf8cc0000000p-3f},{-0x1.f336860000000p-1f,-0x1.c6d9060000000p-3f},
{-0x1.f38f3a0000000p-1f,-0x1.c0b8260000000p-3f},{-0x1.f3e6bc0000000p-1f,-0x1.ba96340000000p-3f},{-0x1.f43d080000000p-1f,-0x1.b4732e0000000p-3f},{-0x1.f492200000000p-1f,-0x1.ae4f1e0000000p-3f},
{-0x1.f4e6040000000p-1f,-0x1.a82a020000000p-3f},{-0x1.f538b20000000p-1f,-0x1.a203e20000000p-3f},{-0x1.f58a2c0000000p-1f,-0x1.9bdcc00000000p-3f},{-0x1.f5da6e0000000p-1f,-0x1.95b49e0000000p-3f},
{-0x1.f6297c0000000p-1f,-0x1.8f8b840000000p-3f},{-0x1.f677560000000p-1f,-0x1.8961720000000p-3f},{-0x1.f6c3f80000000p-1f,-0x1.83366e0000000p-3f},{-0x1.f70f640000000p-1f,-0x1.7d0a7c0000000p-3f},
{-0x1.f7599a0000000p-1f,-0x1.76dd9e0000000p-3f},{-0x1.f7a29a0000000p-1f,-0x1.70afd80000000p-3f},{-0x1.f7ea620000000p-1f,-0x1.6a81300000000p-3f},{-0x1.f830f40000000p-1f,-0x1.6451a80000000p-3f},
{-0x1.f876500000000p-1f,-0x1.5e21440000000p-3f},{-0x1.f8ba740000000p-1f,-0x1.57f0080000000p-3f},{-0x1.f8fd600000000p-1f,-0x1.51bdf80000000p-3f},{-0x1.f93f140000000p-1f,-0x1.4b8b180000000p-3f},
{-0x1.f97f920000000p-1f,-0x1.45576c0000000p-3f},{-0x1.f9bed80000000p-1f,-0x1.3f22f60000000p-3f},{-0x1.f9fce60000000p-1f,-0x1.38edbc0000000p-3f},{-0x1.fa39ba0000000p-1f,-0x1.32b7c00000000p-3f},
{-0x1.fa75580000000p-1f,-0x1.2c81060000000p-3f},{-0x1.faafbc0000000p-1f,-0x1.2649940000000p-3f},{-0x1.fae8e80000000p-1f,-0x1.20116e0000000p-3f},{-0x1.fb20dc0000000p-1f,-0x1.19d8940000000p-3f},
{-0x1.fb57980000000p-1f,-0x1.139f0c0000000p-3f},{-0x1.fb8d180000000p-1f,-0x1.0d64dc0000000p-3f},{-0x1.fbc1620000000p-1f,-0x1.072a040000000p-3f},{-0x1.fbf4700000000p-1f,-0x1.00ee8a0000000p-3f},
{-0x1.fc26480000000p-1f,-0x1.f564e60000000p-4f},{-0x1.fc56e40000000p-1f,-0x1.e8eb800000000p-4f},{-0x1.fc86460000000p-1f,-0x1.dc70ec0000000p-4f},{-0x1.fcb4700000000p-1f,-0x1.cff5340000000p-4f},
{-0x1.fce1600000000p-1f,-0x1.c3785c0000000p-4f},{-0x1.fd0d160000000p-1f,-0x1.b6fa6e0000000p-4f},{-0x1.fd37920000000p-1f,-0x1.aa7b720000000p-4f},{-0x1.fd60d20000000p-1f,-0x1.9dfb6e0000000p-4f},
{-0x1.fd88da0000000p-1f,-0x1.917a6c0000000p-4f},{-0x1.fdafa80000000p-1f,-0x1.84f8720000000p-4f},{-0x1.fdd53a0000000p-1f,-0x1.7875860000000p-4f},{-0x1.fdf9920000000p-1f,-0x1.6bf1b40000000p-4f},
{-0x1.fe1cb00000000p-1f,-0x1.5f6d000000000p-4f},{-0x1.fe3e920000000p-1f,-0x1.52e7740000000p-4f},{-0x1.fe5f3a0000000p-1f,-0x1.4661180000000p-4f},{-0x1.fe7ea80000000p-1f,-0x1.39d9f20000000p-4f},
{-0x1.fe9cda0000000p-1f,-0x1.2d520a0000000p-4f},{-0x1.feb9d20000000p-1f,-0x1.20c9680000000p-4f},{-0x1.fed58e0000000p-1f,-0x1.1440140000000p-4f},{-0x1.fef0100000000p-1f,-0x1.07b6140000000p-4f},
{-0x1.ff09560000000p-1f,-0x1.f656e80000000p-5f},{-0x1.ff21620000000p-1f,-0x1.dd40700000000p-5f},{-0x1.ff38300000000p-1f,-0x1.c428d20000000p-5f},{-0x1.ff4dc60000000p-1f,-0x1.ab101c0000000p-5f},
{-0x1.ff621e0000000p-1f,-0x1.91f6600000000p-5f},{-0x1.ff753c0000000p-1f,-0x1.78dbaa0000000p-5f},{-0x1.ff871e0000000p-1f,-0x1.5fc00e0000000p-5f},{-0x1.ff97c40000000p-1f,-0x1.46a3960000000p-5f},
{-0x1.ffa72e0000000p-1f,-0x1.2d86580000000p-5f},{-0x1.ffb55e0000000p-1f,-0x1.14685e0000000p-5f},{-0x1.ffc2520000000p-1f,-0x1.f693740000000p-6f},{-0x1.ffce0a0000000p-1f,-0x1.c454f40000000p-6f},
{-0x1.ffd8860000000p-1f,-0x1.9215600000000p-6f},{-0x1.ffe1c60000000p-1f,-0x1.5fd4d20000000p-6f},{-0x1.ffe9cc0000000p-1f,-0x1.2d936c0000000p-6f},{-0x1.fff0940000000p-1f,-0x1.f6a2960000000p-7f},
{-0x1.fff6220000000p-1f,-0x1.921d200000000p-7f},{-0x1.fffa720000000p-1f,-0x1.2d96b00000000p-7f},{-0x1.fffd880000000p-1f,-0x1.921f100000000p-8f},{-0x1.ffff620000000p-1f,-0x1.921f8c0000000p-9f},
{-0x1.0000000000000p+0f,0x0.0p+0f},{-0x1.ffff620000000p-1f,0x1.921f8c0000000p-9f},{-0x1.fffd880000000p-1f,0x1.921f100000000p-8f},{-0x1.fffa720000000p-1f,0x1.2d96b00000000p-7f},
{-0x1.fff6220000000p-1f,0x1.921d200000000p-7f},{-0x1.fff0940000000p-1f,0x1.f6a2960000000p-7f},{-0x1.ffe9cc0000000p-1f,0x1.2d936c0000000p-6f},{-0x1.ffe1c60000000p-1f,0x1.5fd4d20000000p-6f},
{-0x1.ffd8860000000p-1f,0x1.9215600000000p-6f},{-0x1.ffce0a0000000p-1f,0x1.c454f40000000p-6f},{-0x1.ffc2520000000p-1f,0x1.f693740000000p-6f},{-0x1.ffb55e0000000p-1f,0x1.14685e0000000p-5f},
{-0x1.ffa72e0000000p-1f,0x1.2d86580000000p-5f},{-0x1.ff97c40000000p-1f,0x1.46a3960000000p-5f},{-0x1.ff871e0000000p-1f,0x1.5fc00e0000000p-5f},{-0x1.ff753c0000000p-1f,0x1.78dbaa0000000p-5f},
{-0x1.ff621e0000000p-1f,0x1.91f6600000000p-5f},{-0x1.ff4dc60000000p-1f,0x1.ab101c0000000p-5f},{-0x1.ff38300000000p-1f,0x1.c428d20000000p-5f},{-0x1.ff21620000000p-1f,0x1.dd40700000000p-5f},
{-0x1.ff09560000000p-1f,0x1.f656e80000000p-5f},{-0x1.fef0100000000p-1f,0x1.07b6140000000p-4f},{-0x1.fed58e0000000p-1f,0x1.1440140000000p-4f},{-0x1.feb9d20000000p-1f,0x1.20c9680000000p-4f},
{-0x1.fe9cda0000000p-1f,0x1.2d520a0000000p-4f},{-0x1.fe7ea80000000p-1f,0x1.39d9f20000000p-4f},{-0x1.fe5f3a0000000p-1f,0x1.4661180000000p-4f},{-0x1.fe3e920000000p-1f,0x1.52e7740000000p-4f},
{-0x1.fe1cb00000000p-1f,0x1.5f6d000000000p-4f},{-0x1.fdf9920000000p-1f,0x1.6bf1b40000000p-4f},{-0x1.fdd53a0000000p-1f,0x1.7875860000000p-4f},{-0x1.fdafa80000000p-1f,0x1.84f8720000000p-4f},
{-0x1.fd88da0000000p-1f,0x1.917a6c0000000p-4f},{-0x1.fd60d20000000p-1f,0x1.9dfb6e0000000p-4f},{-0x1.fd37920000000p-1f,0x1.aa7b720000000p-4f},{-0x1.fd0d160000000p-1f,0x1.b6fa6e0000000p-4f},
{-0x1.fce1600000000p-1f,0x1.c3785c0000000p-4f},{-0x1.fcb4700000000p-1f,0x1.cff5340000000p-4f},{-0x1.fc86460000000p-1f,0x1.dc70ec0000000p-4f},{-0x1.fc56e40000000p-1f,0x1.e8eb800000000p-4f},
{-0x1.fc26480000000p-1f,0x1.f564e60000000p-4f},{-0x1.fbf4700000000p-1f,0x1.00ee8a0000000p-3f},{-0x1.fbc1620000000p-1f,0x1.072a040000000p-3f},{-0x1.fb8d180000000p-1f,0x1.0d64dc0000000p-3f},
{-0x1.fb57980000000p-1f,0x1.139f0c0000000p-3f},{-0x1.fb20dc0000000p-1f,0x1.19d8940000000p-3f},{-0x1.fae8e80000000p-1f,0x1.20116e0000000p-3f},{-0x1.faafbc0000000p-1f,0x1.2649940000000p-3f},
{-0x1.fa75580000000p-1f,0x1.2c81060000000p-3f},{-0x1.fa39ba0000000p-1f,0x1.32b7c00000000p-3f},{-0x1.f9fce60000000p-1f,0x1.38edbc0000000p-3f},{-0x1.f9bed80000000p-1f,0x1.3f22f60000000p-3f},
{-0x1.f97f920000000p-1f,0x1.45576c0000000p-3f},{-0x1.f93f140000000p-1f,0x1.4b8b180000000p-3f},{-0x1.f8fd600000000p-1f,0x1.51bdf80000000p-3f},{-0x1.f8ba740000000p-1f,0x1.57f0080000000p-3f},
{-0x1.f876500000000p-1f,0x1.5e21440000000p-3f},{-0x1.f830f40000000p-1f,0x1.6451a80000000p-3f},{-0x1.f7ea620000000p-1f,0x1.6a81300000000p-3f},{-0x1.f7a29a0000000p-1f,0x1.70afd80000000p-3f},
{-0x1.f7599a0000000p-1f,0x1.76dd9e0000000p-3f},{-0x1.f70f640000000p-1f,0x1.7d0a7c0000000p-3f},{-0x1.f6c3f80000000p-1f,0x1.83366e0000000p-3f},{-0x1.f677560000000p-1f,0x1.8961720000000p-3f},
{-0x1.f6297c0000000p-1f,0x1.8f8b840000000p-3f},{-0x1.f5da6e0000000p-1f,0x1.95b49e0000000p-3f},{-0x1.f58a2c0000000p-1f,0x1.9bdcc00000000p-3f},{-0x1.f538b20000000p-1f,0x1.a203e20000000p-3f},
{-0x1.f4e6040000000p-1f,0x1.a82a020000000p-3f},{-0x1.f492200000000p-1f,0x1.ae4f1e0000000p-3f},{-0x1.f43d080000000p-1f,0x1.b4732e0000000p-3f},{-0x1.f3e6bc0000000p-1f,0x1.ba96340000000p-3f},
{-0x1.f38f3a0000000p-1f,0x1.c0b8260000000p-3f},{-0x1.f336860000000p-1f,0x1.c6d9060000000p-3f},{-0x1.f2dc9c0000000p-1f,0x1.ccf8cc0000000p-3f},{-0x1.f281800000000p-1f,0x1.d317740000000p-3f},
{-0x1.f225300000000p-1f,0x1.d934fe0000000p-3f},{-0x1.f1c7ac0000000p-1f,0x1.df51640000000p-3f},{-0x1.f168f60000000p-1f,0x1.e56ca20000000p-3f},{-0x1.f1090c0000000p-1f,0x1.eb86b40000000p-3f},
{-0x1.f0a7f00000000p-1f,0x1.f19f980000000p-3f},{-0x1.f045a20000000p-1f,0x1.f7b7480000000p-3f},{-0x1.efe2200000000p-1f,0x1.fdcdc20000000p-3f},{-0x1.ef7d6e0000000p-1f,0x1.01f1800000000p-2f},
{-0x1.ef178a0000000p-1f,0x1.04fb800000000p-2f},{-0x1.eeb0740000000p-1f,0x1.0804e00000000p-2f},{-0x1.ee482e0000000p-1f,0x1.0b0d9c0000000p-2f},{-0x1.eddeb60000000p-1f,0x1.0e15b40000000p-2f},
{-0x1.ed740e0000000p-1f,0x1.111d260000000p-2f},{-0x1.ed08360000000p-1f,0x1.1423ee0000000p-2f},{-0x1.ec9b2e0000000p-1f,0x1.172a0e0000000p-2f},{-0x1.ec2cf40000000p-1f,0x1.1a2f800000000p-2f},
{-0x1.ebbd8c0000000p-1f,0x1.1d34440000000p-2f},{-0x1.eb4cf60000000p-1f,0x1.2038580000000p-2f},{-0x1.eadb2e0000000p-1f,0x1.233bba0000000p-2f},{-0x1.ea683a0000000p-1f,0x1.263e6a0000000p-2f},
{-0x1.e9f4160000000p-1f,0x1.2940620000000p-2f},{-0x1.e97ec40000000p-1f,0x1.2c41a40000000p-2f},{-0x1.e908440000000p-1f,0x1.2f422e0000000p-2f},{-0x1.e890960000000p-1f,0x1.3241fc0000000p-2f},
{-0x1.e817ba0000000p-1f,0x1.35410c0000000p-2f},{-0x1.e79db20000000p-1f,0x1.383f5e0000000p-2f},{-0x1.e7227e0000000p-1f,0x1.3b3cf00000000p-2f},{-0x1.e6a61c0000000p-1f,0x1.3e39be0000000p-2f},
{-0x1.e6288e0000000p-1f,0x1.4135ca0000000p-2f},{-0x1.e5a9d60000000p-1f,0x1.44310e0000000p-2f},{-0x1.e529f00000000p-1f,0x1.472b8a0000000p-2f},{-0x1.e4a8e00000000p-1f,0x1.4a253e0000000p-2f},
{-0x1.e426a40000000p-1f,0x1.4d1e240000000p-2f},{-0x1.e3a33e0000000p-1f,0x1.50163e0000000p-2f},{-0x1.e31eae0000000p-1f,0x1.530d880000000p-2f},{-0x1.e298f40000000p-1f,0x1.5604020000000p-2f},
{-0x1.e212100000000p-1f,0x1.58f9a80000000p-2f},{-0x1.e18a020000000p-1f,0x1.5bee780000000p-2f},{-0x1.e100cc0000000p-1f,0x1.5ee2740000000p-2f},{-0x1.e0766e0000000p-1f,0x1.61d5960000000p-2f},
{-0x1.dfeae60000000p-1f,0x1.64c7de0000000p-2f},{-0x1.df5e360000000p-1f,0x1.67b94a0000000p-2f},{-0x1.ded0600000000p-1f,0x1.6aa9d80000000p-2f},{-0x1.de41600000000p-1f,0x1.6d99860000000p-2f},
{-0x1.ddb13c0000000p-1f,0x1.7088540000000p-2f},{-0x1.dd1ff00000000p-1f,0x1.73763c0000000p-2f},{-0x1.dc8d7c0000000p-1f,0x1.7663400000000p-2f},{-0x1.dbf9e40000000p-1f,0x1.794f5e0000000p-2f},
{-0x1.db65260000000p-1f,0x1.7c3a940000000p-2f},{-0x1.dacf420000000p-1f,0x1.7f24de0000000p-2f},{-0x1.da383a0000000p-1f,0x1.820e3c0000000p-2f},{-0x1.d9a00e0000000p-1f,0x1.84f6aa0000000p-2f},
{-0x1.d906bc0000000p-1f,0x1.87de2a0000000p-2f},{-0x1.d86c480000000p-1f,0x1.8ac4b80000000p-2f},{-0x1.d7d0b00000000p-1f,0x1.8daa520000000p-2f},{-0x1.d733f60000000p-1f,0x1.908ef80000000p-2f},
{-0x1.d696180000000p-1f,0x1.9372a60000000p-2f},{-0x1.d5f7180000000p-1f,0x1.96555c0000000p-2f},{-0x1.d556f60000000p-1f,0x1.9937160000000p-2f},{-0x1.d4b5b20000000p-1f,0x1.9c17d40000000p-2f},
{-0x1.d4134e0000000p-1f,0x1.9ef7940000000p-2f},{-0x1.d36fc80000000p-1f,0x1.a1d6540000000p-2f},{-0x1.d2cb220000000p-1f,0x1.a4b4120000000p-2f},{-0x1.d2255c0000000p-1f,0x1.a790ce0000000p-2f},
{-0x1.d17e780000000p-1f,0x1.aa6c820000000p-2f},{-0x1.d0d6720000000p-1f,0x1.ad47320000000p-2f},{-0x1.d02d500000000p-1f,0x1.b020d60000000p-2f},{-0x1.cf830e0000000p-1f,0x1.b2f9720000000p-2f},
{-0x1.ced7b00000000p-1f,0x1.b5d1000000000p-2f},{-0x1.ce2b320000000p-1f,0x1.b8a7820000000p-2f},{-0x1.cd7d980000000p-1f,0x1.bb7cf20000000p-2f},{-0x1.cccee20000000p-1f,0x1.be51520000000p-2f},
{-0x1.cc1f100000000p-1f,0x1.c1249e0000000p-2f},{-0x1.cb6e200000000p-1f,0x1.c3f6d40000000p-2f},{-0x1.cabc160000000p-1f,0x1.c6c7f40000000p-2f},{-0x1.ca08f20000000p-1f,0x1.c997fc0000000p-2f},
{-0x1.c954b20000000p-1f,0x1.cc66ea0000000p-2f},{-0x1.c89f580000000p-1f,0x1.cf34ba0000000p-2f},{-0x1.c7e8e60000000p-1f,0x1.d2016e0000000p-2f},{-0x1.c731580000000p-1f,0x1.d4cd020000000p-2f},
{-0x1.c678b40000000p-1f,0x1.d797760000000p-2f},{-0x1.c5bef60000000p-1f,0x1.da60c60000000p-2f},{-0x1.c504200000000p-1f,0x1.dd28f20000000p-2f},{-0x1.c448340000000p-1f,0x1.dfeff60000000p-2f},
{-0x1.c38b300000000p-1f,0x1.e2b5d40000000p-2f},{-0x1.c2cd140000000p-1f,0x1.e57a860000000p-2f},{-0x1.c20de40000000p-1f,0x1.e83e0e0000000p-2f},{-0x1.c14d9e0000000p-1f,0x1.eb006a0000000p-2f},
{-0x1.c08c420000000p-1f,0x1.edc1960000000p-2f},{-0x1.bfc9d20000000p-1f,0x1.f081900000000p-2f},{-0x1.bf064e0000000p-1f,0x1.f3405a0000000p-2f},{-0x1.be41b60000000p-1f,0x1.f5fdee0000000p-2f},
{-0x1.bd7c0a0000000p-1f,0x1.f8ba4e0000000p-2f},{-0x1.bcb54c0000000p-1f,0x1.fb75760000000p-2f},{-0x1.bbed7c0000000p-1f,0x1.fe2f640000000p-2f},{-0x1.bb249a0000000p-1f,0x1.00740c0000000p-1f},
{-0x1.ba5aa60000000p-1f,0x1.01cfc80000000p-1f},{-0x1.b98fa20000000p-1f,0x1.032ae60000000p-1f},{-0x1.b8c38e0000000p-1f,0x1.0485620000000p-1f},{-0x1.b7f6680000000p-1f,0x1.05df3e0000000p-1f},
{-0x1.b728340000000p-1f,0x1.07387a0000000p-1f},{-0x1.b658f20000000p-1f,0x1.0891120000000p-1f},{-0x1.b588a00000000p-1f,0x1.09e9080000000p-1f},{-0x1.b4b7400000000p-1f,0x1.0b40580000000p-1f},
{-0x1.b3e4d40000000p-1f,0x1.0c97040000000p-1f},{-0x1.b3115a0000000p-1f,0x1.0ded0c0000000p-1f},{-0x1.b23cd40000000p-1f,0x1.0f426c0000000p-1f},{-0x1.b167420000000p-1f,0x1.1097240000000p-1f},
{-0x1.b090a60000000p-1f,0x1.11eb360000000p-1f},{-0x1.afb8fe0000000p-1f,0x1.133e9c0000000p-1f},{-0x1.aee04c0000000p-1f,0x1.14915a0000000p-1f},{-0x1.ae06900000000p-1f,0x1.15e36e0000000p-1f},
{-0x1.ad2bca0000000p-1f,0x1.1734d60000000p-1f},{-0x1.ac4ffc0000000p-1f,0x1.1885920000000p-1f},{-0x1.ab73260000000p-1f,0x1.19d5a00000000p-1f},{-0x1.aa95480000000p-1f,0x1.1b25020000000p-1f},
{-0x1.a9b6620000000p-1f,0x1.1c73b40000000p-1f},{-0x1.a8d6760000000p-1f,0x1.1dc1b60000000p-1f},{-0x1.a7f5860000000p-1f,0x1.1f0f080000000p-1f},{-0x1.a7138e0000000p-1f,0x1.205baa0000000p-1f},
{-0x1.a630920000000p-1f,0x1.21a79a0000000p-1f},{-0x1.a54c920000000p-1f,0x1.22f2d60000000p-1f},{-0x1.a4678c0000000p-1f,0x1.243d600000000p-1f},{-0x1.a381840000000p-1f,0x1.2587340000000p-1f},
{-0x1.a29a7a0000000p-1f,0x1.26d0540000000p-1f},{-0x1.a1b26e0000000p-1f,0x1.2818be0000000p-1f},{-0x1.a0c95e0000000p-1f,0x1.2960720000000p-1f},{-0x1.9fdf500000000p-1f,0x1.2aa76e0000000p-1f},
{-0x1.9ef43e0000000p-1f,0x1.2bedb20000000p-1f},{-0x1.9e082e0000000p-1f,0x1.2d333e0000000p-1f},{-0x1.9d1b200000000p-1f,0x1.2e780e0000000p-1f},{-0x1.9c2d120000000p-1f,0x1.2fbc240000000p-1f},
{-0x1.9b3e040000000p-1f,0x1.30ff800000000p-1f},{-0x1.9a4dfa0000000p-1f,0x1.32421e0000000p-1f},{-0x1.995cf20000000p-1f,0x1.3384000000000p-1f},{-0x1.986af00000000p-1f,0x1.34c5260000000p-1f},
{-0x1.9777f00000000p-1f,0x1.36058c0000000p-1f},{-0x1.9683f40000000p-1f,0x1.3745320000000p-1f},{-0x1.958efe0000000p-1f,0x1.3884180000000p-1f},{-0x1.94990e0000000p-1f,0x1.39c23e0000000p-1f},
{-0x1.93a2240000000p-1f,0x1.3affa20000000p-1f},{-0x1.92aa420000000p-1f,0x1.3c3c440000000p-1f},{-0x1.91b1660000000p-1f,0x1.3d78240000000p-1f},{-0x1.90b7940000000p-1f,0x1.3eb33e0000000p-1f},
{-0x1.8fbcca0000000p-1f,0x1.3fed960000000p-1f},{-0x1.8ec10a0000000p-1f,0x1.4127260000000p-1f},{-0x1.8dc4540000000p-1f,0x1.425ff20000000p-1f},{-0x1.8cc6a80000000p-1f,0x1.4397f60000000p-1f},
{-0x1.8bc8060000000p-1f,0x1.44cf320000000p-1f},{-0x1.8ac8720000000p-1f,0x1.4605a60000000p-1f},{-0x1.89c7ea0000000p-1f,0x1.473b520000000p-1f},{-0x1.88c66e0000000p-1f,0x1.4870340000000p-1f},
{-0x1.87c4000000000p-1f,0x1.49a44a0000000p-1f},{-0x1.86c0a20000000p-1f,0x1.4ad7960000000p-1f},{-0x1.85bc520000000p-1f,0x1.4c0a140000000p-1f},{-0x1.84b7120000000p-1f,0x1.4d3bc60000000p-1f},
{-0x1.83b0e00000000p-1f,0x1.4e6cac0000000p-1f},{-0x1.82a9c20000000p-1f,0x1.4f9cc20000000p-1f},{-0x1.81a1b40000000p-1f,0x1.50cc0a0000000p-1f},{-0x1.8098b80000000p-1f,0x1.51fa820000000p-1f},
{-0x1.7f8ece0000000p-1f,0x1.53282a0000000p-1f},{-0x1.7e83f80000000p-1f,0x1.5455000000000p-1f},{-0x1.7d78360000000p-1f,0x1.5581040000000p-1f},{-0x1.7c6b8a0000000p-1f,0x1.56ac360000000p-1f},
{-0x1.7b5df20000000p-1f,0x1.57d6940000000p-1f},{-0x1.7a4f700000000p-1f,0x1.59001e0000000p-1f},{-0x1.7940060000000p-1f,0x1.5a28d20000000p-1f},{-0x1.782fb20000000p-1f,0x1.5b50b20000000p-1f},
{-0x1.771e760000000p-1f,0x1.5c77bc0000000p-1f},{-0x1.760c520000000p-1f,0x1.5d9dee0000000p-1f},{-0x1.74f9480000000p-1f,0x1.5ec34a0000000p-1f},{-0x1.73e5580000000p-1f,0x1.5fe7cc0000000p-1f},
{-0x1.72d0840000000p-1f,0x1.610b760000000p-1f},{-0x1.71baca0000000p-1f,0x1.622e440000000p-1f},{-0x1.70a42c0000000p-1f,0x1.63503a0000000p-1f},{-0x1.6f8caa0000000p-1f,0x1.6471540000000p-1f},
{-0x1.6e74460000000p-1f,0x1.6591920000000p-1f},{-0x1.6d5afe0000000p-1f,0x1.66b0f40000000p-1f},{-0x1.6c40d80000000p-1f,0x1.67cf780000000p-1f},{-0x1.6b25ce0000000p-1f,0x1.68ed1e0000000p-1f},
{-0x1.6a09e60000000p-1f,0x1.6a09e60000000p-1f},{-0x1.68ed1e0000000p-1f,0x1.6b25ce0000000p-1f},{-0x1.67cf780000000p-1f,0x1.6c40d80000000p-1f},{-0x1.66b0f40000000p-1f,0x1.6d5afe0000000p-1f},
{-0x1.6591920000000p-1f,0x1.6e74460000000p-1f},{-0x1.6471540000000p-1f,0x1.6f8caa0000000p-1f},{-0x1.63503a0000000p-1f,0x1.70a42c0000000p-1f},{-0x1.622e440000000p-1f,0x1.71baca0000000p-1f},
{-0x1.610b760000000p-1f,0x1.72d0840000000p-1f},{-0x1.5fe7cc0000000p-1f,0x1.73e5580000000p-1f},{-0x1.5ec34a0000000p-1f,0x1.74f9480000000p-1f},{-0x1.5d9dee0000000p-1f,0x1.760c520000000p-1f},
{-0x1.5c77bc0000000p-1f,0x1.771e760000000p-1f},{-0x1.5b50b20000000p-1f,0x1.782fb20000000p-1f},{-0x1.5a28d20000000p-1f,0x1.7940060000000p-1f},{-0x1.59001e0000000p-1f,0x1.7a4f700000000p-1f},
{-0x1.57d6940000000p-1f,0x1.7b5df20000000p-1f},{-0x1.56ac360000000p-1f,0x1.7c6b8a0000000p-1f},{-0x1.5581040000000p-1f,0x1.7d78360000000p-1f},{-0x1.5455000000000p-1f,0x1.7e83f80000000p-1f},
{-0x1.53282a0000000p-1f,0x1.7f8ece0000000p-1f},{-0x1.51fa820000000p-1f,0x1.8098b80000000p-1f},{-0x1.50cc0a0000000p-1f,0x1.81a1b40000000p-1f},{-0x1.4f9cc20000000p-1f,0x1.82a9c20000000p-1f},
{-0x1.4e6cac0000000p-1f,0x1.83b0e00000000p-1f},{-0x1.4d3bc60000000p-1f,0x1.84b7120000000p-1f},{-0x1.4c0a140000000p-1f,0x1.85bc520000000p-1f},{-0x1.4ad7960000000p-1f,0x1.86c0a20000000p-1f},
{-0x1.49a44a0000000p-1f,0x1.87c4000000000p-1f},{-0x1.4870340000000p-1f,0x1.88c66e0000000p-1f},{-0x1.473b520000000p-1f,0x1.89c7ea0000000p-1f},{-0x1.4605a60000000p-1f,0x1.8ac8720000000p-1f},
{-0x1.44cf320000000p-1f,0x1.8bc8060000000p-1f},{-0x1.4397f60000000p-1f,0x1.8cc6a80000000p-1f},{-0x1.425ff20000000p-1f,0x1.8dc4540000000p-1f},{-0x1.4127260000000p-1f,0x1.8ec10a0000000p-1f},
{-0x1.3fed960000000p-1f,0x1.8fbcca0000000p-1f},{-0x1.3eb33e0000000p-1f,0x1.90b7940000000p-1f},{-0x1.3d78240000000p-1f,0x1.91b1660000000p-1f},{-0x1.3c3c440000000p-1f,0x1.92aa420000000p-1f},
{-0x1.3affa20000000p-1f,0x1.93a2240000000p-1f},{-0x1.39c23e0000000p-1f,0x1.94990e0000000p-1f},{-0x1.3884180000000p-1f,0x1.958efe0000000p-1f},{-0x1.3745320000000p-1f,0x1.9683f40000000p-1f},
{-0x1.36058c0000000p-1f,0x1.9777f00000000p-1f},{-0x1.34c5260000000p-1f,0x1.986af00000000p-1f},{-0x1.3384000000000p-1f,0x1.995cf20000000p-1f},{-0x1.32421e0000000p-1f,0x1.9a4dfa0000000p-1f},
{-0x1.30ff800000000p-1f,0x1.9b3e040000000p-1f},{-0x1.2fbc240000000p-1f,0x1.9c2d120000000p-1f},{-0x1.2e780e0000000p-1f,0x1.9d1b200000000p-1f},{-0x1.2d333e0000000p-1f,0x1.9e082e0000000p-1f},
{-0x1.2bedb20000000p-1f,0x1.9ef43e0000000p-1f},{-0x1.2aa76e0000000p-1f,0x1.9fdf500000000p-1f},{-0x1.2960720000000p-1f,0x1.a0c95e0000000p-1f},{-0x1.2818be0000000p-1f,0x1.a1b26e0000000p-1f},
{-0x1.26d0540000000p-1f,0x1.a29a7a0000000p-1f},{-0x1.2587340000000p-1f,0x1.a381840000000p-1f},{-0x1.243d600000000p-1f,0x1.a4678c0000000p-1f},{-0x1.22f2d60000000p-1f,0x1.a54c920000000p-1f},
{-0x1.21a79a0000000p-1f,0x1.a630920000000p-1f},{-0x1.205baa0000000p-1f,0x1.a7138e0000000p-1f},{-0x1.1f0f080000000p-1f,0x1.a7f5860000000p-1f},{-0x1.1dc1b60000000p-1f,0x1.a8d6760000000p-1f},
{-0x1.1c73b40000000p-1f,0x1.a9b6620000000p-1f},{-0x1.1b25020000000p-1f,0x1.aa95480000000p-1f},{-0x1.19d5a00000000p-1f,0x1.ab73260000000p-1f},{-0x1.1885920000000p-1f,0x1.ac4ffc0000000p-1f},
{-0x1.1734d60000000p-1f,0x1.ad2bca0000000p-1f},{-0x1.15e36e0000000p-1f,0x1.ae06900000000p-1f},{-0x1.14915a0000000p-1f,0x1.aee04c0000000p-1f},{-0x1.133e9c0000000p-1f,0x1.afb8fe0000000p-1f},
{-0x1.11eb360000000p-1f,0x1.b090a60000000p-1f},{-0x1.1097240000000p-1f,0x1.b167420000000p-1f},{-0x1.0f426c0000000p-1f,0x1.b23cd40000000p-1f},{-0x1.0ded0c0000000p-1f,0x1.b3115a0000000p-1f},
{-0x1.0c97040000000p-1f,0x1.b3e4d40000000p-1f},{-0x1.0b40580000000p-1f,0x1.b4b7400000000p-1f},{-0x1.09e9080000000p-1f,0x1.b588a00000000p-1f},{-0x1.0891120000000p-1f,0x1.b658f20000000p-1f},
{-0x1.07387a0000000p-1f,0x1.b728340000000p-1f},{-0x1.05df3e0000000p-1f,0x1.b7f6680000000p-1f},{-0x1.0485620000000p-1f,0x1.b8c38e0000000p-1f},{-0x1.032ae60000000p-1f,0x1.b98fa20000000p-1f},
{-0x1.01cfc80000000p-1f,0x1.ba5aa60000000p-1f},{-0x1.00740c0000000p-1f,0x1.bb249a0000000p-1f},{-0x1.fe2f640000000p-2f,0x1.bbed7c0000000p-1f},{-0x1.fb75760000000p-2f,0x1.bcb54c0000000p-1f},
{-0x1.f8ba4e0000000p-2f,0x1.bd7c0a0000000p-1f},{-0x1.f5fdee0000000p-2f,0x1.be41b60000000p-1f},{-0x1.f3405a0000000p-2f,0x1.bf064e0000000p-1f},{-0x1.f081900000000p-2f,0x1.bfc9d20000000p-1f},
{-0x1.edc1960000000p-2f,0x1.c08c420000000p-1f},{-0x1.eb006a0000000p-2f,0x1.c14d9e0000000p-1f},{-0x1.e83e0e0000000p-2f,0x1.c20de40000000p-1f},{-0x1.e57a860000000p-2f,0x1.c2cd140000000p-1f},
{-0x1.e2b5d40000000p-2f,0x1.c38b300000000p-1f},{-0x1.dfeff60000000p-2f,0x1.c448340000000p-1f},{-0x1.dd28f20000000p-2f,0x1.c504200000000p-1f},{-0x1.da60c60000000p-2f,0x1.c5bef60000000p-1f},
{-0x1.d797760000000p-2f,0x1.c678b40000000p-1f},{-0x1.d4cd020000000p-2f,0x1.c731580000000p-1f},{-0x1.d2016e0000000p-2f,0x1.c7e8e60000000p-1f},{-0x1.cf34ba0000000p-2f,0x1.c89f580000000p-1f},
{-0x1.cc66ea0000000p-2f,0x1.c954b20000000p-1f},{-0x1.c997fc0000000p-2f,0x1.ca08f20000000p-1f},{-0x1.c6c7f40000000p-2f,0x1.cabc160000000p-1f},{-0x1.c3f6d40000000p-2f,0x1.cb6e200000000p-1f},
{-0x1.c1249e0000000p-2f,0x1.cc1f100000000p-1f},{-0x1.be51520000000p-2f,0x1.cccee20000000p-1f},{-0x1.bb7cf20000000p-2f,0x1.cd7d980000000p-1f},{-0x1.b8a7820000000p-2f,0x1.ce2b320000000p-1f},
{-0x1.b5d1000000000p-2f,0x1.ced7b00000000p-1f},{-0x1.b2f9720000000p-2f,0x1.cf830e0000000p-1f},{-0x1.b020d60000000p-2f,0x1.d02d500000000p-1f},{-0x1.ad47320000000p-2f,0x1.d0d6720000000p-1f},
{-0x1.aa6c820000000p-2f,0x1.d17e780000000p-1f},{-0x1.a790ce0000000p-2f,0x1.d2255c0000000p-1f},{-0x1.a4b4120000000p-2f,0x1.d2cb220000000p-1f},{-0x1.a1d6540000000p-2f,0x1.d36fc80000000p-1f},
{-0x1.9ef7940000000p-2f,0x1.d4134e0000000p-1f},{-0x1.9c17d40000000p-2f,0x1.d4b5b20000000p-1f},{-0x1.9937160000000p-2f,0x1.d556f60000000p-1f},{-0x1.96555c0000000p-2f,0x1.d5f7180000000p-1f},
{-0x1.9372a60000000p-2f,0x1.d696180000000p-1f},{-0x1.908ef80000000p-2f,0x1.d733f60000000p-1f},{-0x1.8daa520000000p-2f,0x1.d7d0b00000000p-1f},{-0x1.8ac4b80000000p-2f,0x1.d86c480000000p-1f},
{-0x1.87de2a0000000p-2f,0x1.d906bc0000000p-1f},{-0x1.84f6aa0000000p-2f,0x1.d9a00e0000000p-1f},{-0x1.820e3c0000000p-2f,0x1.da383a0000000p-1f},{-0x1.7f24de0000000p-2f,0x1.dacf420000000p-1f},
{-0x1.7c3a940000000p-2f,0x1.db65260000000p-1f},{-0x1.794f5e0000000p-2f,0x1.dbf9e40000000p-1f},{-0x1.7663400000000p-2f,0x1.dc8d7c0000000p-1f},{-0x1.73763c0000000p-2f,0x1.dd1ff00000000p-1f},
{-0x1.7088540000000p-2f,0x1.ddb13c0000000p-1f},{-0x1.6d99860000000p-2f,0x1.de41600000000p-1f},{-0x1.6aa9d80000000p-2f,0x1.ded0600000000p-1f},{-0x1.67b94a0000000p-2f,0x1.df5e360000000p-1f},
{-0x1.64c7de0000000p-2f,0x1.dfeae60000000p-1f},{-0x1.61d5960000000p-2f,0x1.e0766e0000000p-1f},{-0x1.5ee2740000000p-2f,0x1.e100cc0000000p-1f},{-0x1.5bee780000000p-2f,0x1.e18a020000000p-1f},
{-0x1.58f9a80000000p-2f,0x1.e212100000000p-1f},{-0x1.5604020000000p-2f,0x1.e298f40000000p-1f},{-0x1.530d880000000p-2f,0x1.e31eae0000000p-1f},{-0x1.50163e0000000p-2f,0x1.e3a33e0000000p-1f},
{-0x1.4d1e240000000p-2f,0x1.e426a40000000p-1f},{-0x1.4a253e0000000p-2f,0x1.e4a8e00000000p-1f},{-0x1.472b8a0000000p-2f,0x1.e529f00000000p-1f},{-0x1.44310e0000000p-2f,0x1.e5a9d60000000p-1f},
{-0x1.4135ca0000000p-2f,0x1.e6288e0000000p-1f},{-0x1.3e39be0000000p-2f,0x1.e6a61c0000000p-1f},{-0x1.3b3cf00000000p-2f,0x1.e7227e0000000p-1f},{-0x1.383f5e0000000p-2f,0x1.e79db20000000p-1f},
{-0x1.35410c0000000p-2f,0x1.e817ba0000000p-1f},{-0x1.3241fc0000000p-2f,0x1.e890960000000p-1f},{-0x1.2f422e0000000p-2f,0x1.e908440000000p-1f},{-0x1.2c41a40000000p-2f,0x1.e97ec40000000p-1f},
{-0x1.2940620000000p-2f,0x1.e9f4160000000p-1f},{-0x1.263e6a0000000p-2f,0x1.ea683a0000000p-1f},{-0x1.233bba0000000p-2f,0x1.eadb2e0000000p-1f},{-0x1.2038580000000p-2f,0x1.eb4cf60000000p-1f},
{-0x1.1d34440000000p-2f,0x1.ebbd8c0000000p-1f},{-0x1.1a2f800000000p-2f,0x1.ec2cf40000000p-1f},{-0x1.172a0e0000000p-2f,0x1.ec9b2e0000000p-1f},{-0x1.1423ee0000000p-2f,0x1.ed08360000000p-1f},
{-0x1.111d260000000p-2f,0x1.ed740e0000000p-1f},{-0x1.0e15b40000000p-2f,0x1.eddeb60000000p-1f},{-0x1.0b0d9c0000000p-2f,0x1.ee482e0000000p-1f},{-0x1.0804e00000000p-2f,0x1.eeb0740000000p-1f},
{-0x1.04fb800000000p-2f,0x1.ef178a0000000p-1f},{-0x1.01f1800000000p-2f,0x1.ef7d6e0000000p-1f},{-0x1.fdcdc20000000p-3f,0x1.efe2200000000p-1f},{-0x1.f7b7480000000p-3f,0x1.f045a20000000p-1f},
{-0x1.f19f980000000p-3f,0x1.f0a7f00000000p-1f},{-0x1.eb86b40000000p-3f,0x1.f1090c0000000p-1f},{-0x1.e56ca20000000p-3f,0x1.f168f60000000p-1f},{-0x1.df51640000000p-3f,0x1.f1c7ac0000000p-1f},
{-0x1.d934fe0000000p-3f,0x1.f225300000000p-1f},{-0x1.d317740000000p-3f,0x1.f281800000000p-1f},{-0x1.ccf8cc0000000p-3f,0x1.f2dc9c0000000p-1f},{-0x1.c6d9060000000p-3f,0x1.f336860000000p-1f},
{-0x1.c0b8260000000p-3f,0x1.f38f3a0000000p-1f},{-0x1.ba96340000000p-3f,0x1.f3e6bc0000000p-1f},{-0x1.b4732e0000000p-3f,0x1.f43d080000000p-1f},{-0x1.ae4f1e0000000p-3f,0x1.f492200000000p-1f},
{-0x1.a82a020000000p-3f,0x1.f4e6040000000p-1f},{-0x1.a203e20000000p-3f,0x1.f538b20000000p-1f},{-0x1.9bdcc00000000p-3f,0x1.f58a2c0000000p-1f},{-0x1.95b49e0000000p-3f,0x1.f5da6e0000000p-1f},
{-0x1.8f8b840000000p-3f,0x1.f6297c0000000p-1f},{-0x1.8961720000000p-3f,0x1.f677560000000p-1f},{-0x1.83366e0000000p-3f,0x1.f6c3f80000000p-1f},{-0x1.7d0a7c0000000p-3f,0x1.f70f640000000p-1f},
{-0x1.76dd9e0000000p-3f,0x1.f7599a0000000p-1f},{-0x1.70afd80000000p-3f,0x1.f7a29a0000000p-1f},{-0x1.6a81300000000p-3f,0x1.f7ea620000000p-1f},{-0x1.6451a80000000p-3f,0x1.f830f40000000p-1f},
{-0x1.5e21440000000p-3f,0x1.f876500000000p-1f},{-0x1.57f0080000000p-3f,0x1.f8ba740000000p-1f},{-0x1.51bdf80000000p-3f,0x1.f8fd600000000p-1f},{-0x1.4b8b180000000p-3f,0x1.f93f140000000p-1f},
{-0x1.45576c0000000p-3f,0x1.f97f920000000p-1f},{-0x1.3f22f60000000p-3f,0x1.f9bed80000000p-1f},{-0x1.38edbc0000000p-3f,0x1.f9fce60000000p-1f},{-0x1.32b7c00000000p-3f,0x1.fa39ba0000000p-1f},
{-0x1.2c81060000000p-3f,0x1.fa75580000000p-1f},{-0x1.2649940000000p-3f,0x1.faafbc0000000p-1f},{-0x1.20116e0000000p-3f,0x1.fae8e80000000p-1f},{-0x1.19d8940000000p-3f,0x1.fb20dc0000000p-1f},
{-0x1.139f0c0000000p-3f,0x1.fb57980000000p-1f},{-0x1.0d64dc0000000p-3f,0x1.fb8d180000000p-1f},{-0x1.072a040000000p-3f,0x1.fbc1620000000p-1f},{-0x1.00ee8a0000000p-3f,0x1.fbf4700000000p-1f},
{-0x1.f564e60000000p-4f,0x1.fc26480000000p-1f},{-0x1.e8eb800000000p-4f,0x1.fc56e40000000p-1f},{-0x1.dc70ec0000000p-4f,0x1.fc86460000000p-1f},{-0x1.cff5340000000p-4f,0x1.fcb4700000000p-1f},
{-0x1.c3785c0000000p-4f,0x1.fce1600000000p-1f},{-0x1.b6fa6e0000000p-4f,0x1.fd0d160000000p-1f},{-0x1.aa7b720000000p-4f,0x1.fd37920000000p-1f},{-0x1.9dfb6e0000000p-4f,0x1.fd60d20000000p-1f},
{-0x1.917a6c0000000p-4f,0x1.fd88da0000000p-1f},{-0x1.84f8720000000p-4f,0x1.fdafa80000000p-1f},{-0x1.7875860000000p-4f,0x1.fdd53a0000000p-1f},{-0x1.6bf1b40000000p-4f,0x1.fdf9920000000p-1f},
{-0x1.5f6d000000000p-4f,0x1.fe1cb00000000p-1f},{-0x1.52e7740000000p-4f,0x1.fe3e920000000p-1f},{-0x1.4661180000000p-4f,0x1.fe5f3a0000000p-1f},{-0x1.39d9f20000000p-4f,0x1.fe7ea80000000p-1f},
{-0x1.2d520a0000000p-4f,0x1.fe9cda0000000p-1f},{-0x1.20c9680000000p-4f,0x1.feb9d20000000p-1f},{-0x1.1440140000000p-4f,0x1.fed58e0000000p-1f},{-0x1.07b6140000000p-4f,0x1.fef0100000000p-1f},
{-0x1.f656e80000000p-5f,0x1.ff09560000000p-1f},{-0x1.dd40700000000p-5f,0x1.ff21620000000p-1f},{-0x1.c428d20000000p-5f,0x1.ff38300000000p-1f},{-0x1.ab101c0000000p-5f,0x1.ff4dc60000000p-1f},
{-0x1.91f6600000000p-5f,0x1.ff621e0000000p-1f},{-0x1.78dbaa0000000p-5f,0x1.ff753c0000000p-1f},{-0x1.5fc00e0000000p-5f,0x1.ff871e0000000p-1f},{-0x1.46a3960000000p-5f,0x1.ff97c40000000p-1f},
{-0x1.2d86580000000p-5f,0x1.ffa72e0000000p-1f},{-0x1.14685e0000000p-5f,0x1.ffb55e0000000p-1f},{-0x1.f693740000000p-6f,0x1.ffc2520000000p-1f},{-0x1.c454f40000000p-6f,0x1.ffce0a0000000p-1f},
{-0x1.9215600000000p-6f,0x1.ffd8860000000p-1f},{-0x1.5fd4d20000000p-6f,0x1.ffe1c60000000p-1f},{-0x1.2d936c0000000p-6f,0x1.ffe9cc0000000p-1f},{-0x1.f6a2960000000p-7f,0x1.fff0940000000p-1f},
{-0x1.921d200000000p-7f,0x1.fff6220000000p-1f},{-0x1.2d96b00000000p-7f,0x1.fffa720000000p-1f},{-0x1.921f100000000p-8f,0x1.fffd880000000p-1f},{-0x1.921f8c0000000p-9f,0x1.ffff620000000p-1f},
{0x0.0p+0f,0x1.0000000000000p+0f},{0x1.921f8c0000000p-9f,0x1.ffff620000000p-1f},{0x1.921f100000000p-8f,0x1.fffd880000000p-1f},{0x1.2d96b00000000p-7f,0x1.fffa720000000p-1f},
{0x1.921d200000000p-7f,0x1.fff6220000000p-1f},{0x1.f6a2960000000p-7f,0x1.fff0940000000p-1f},{0x1.2d936c0000000p-6f,0x1.ffe9cc0000000p-1f},{0x1.5fd4d20000000p-6f,0x1.ffe1c60000000p-1f},
{0x1.9215600000000p-6f,0x1.ffd8860000000p-1f},{0x1.c454f40000000p-6f,0x1.ffce0a0000000p-1f},{0x1.f693740000000p-6f,0x1.ffc2520000000p-1f},{0x1.14685e0000000p-5f,0x1.ffb55e0000000p-1f},
{0x1.2d86580000000p-5f,0x1.ffa72e0000000p-1f},{0x1.46a3960000000p-5f,0x1.ff97c40000000p-1f},{0x1.5fc00e0000000p-5f,0x1.ff871e0000000p-1f},{0x1.78dbaa0000000p-5f,0x1.ff753c0000000p-1f},
{0x1.91f6600000000p-5f,0x1.ff621e0000000p-1f},{0x1.ab101c0000000p-5f,0x1.ff4dc60000000p-1f},{0x1.c428d20000000p-5f,0x1.ff38300000000p-1f},{0x1.dd40700000000p-5f,0x1.ff21620000000p-1f},
{0x1.f656e80000000p-5f,0x1.ff09560000000p-1f},{0x1.07b6140000000p-4f,0x1.fef0100000000p-1f},{0x1.1440140000000p-4f,0x1.fed58e0000000p-1f},{0x1.20c9680000000p-4f,0x1.feb9d20000000p-1f},
{0x1.2d520a0000000p-4f,0x1.fe9cda0000000p-1f},{0x1.39d9f20000000p-4f,0x1.fe7ea80000000p-1f},{0x1.4661180000000p-4f,0x1.fe5f3a0000000p-1f},{0x1.52e7740000000p-4f,0x1.fe3e920000000p-1f},
{0x1.5f6d000000000p-4f,0x1.fe1cb00000000p-1f},{0x1.6bf1b40000000p-4f,0x1.fdf9920000000p-1f},{0x1.7875860000000p-4f,0x1.fdd53a0000000p-1f},{0x1.84f8720000000p-4f,0x1.fdafa80000000p-1f},
{0x1.917a6c0000000p-4f,0x1.fd88da0000000p-1f},{0x1.9dfb6e0000000p-4f,0x1.fd60d20000000p-1f},{0x1.aa7b720000000p-4f,0x1.fd37920000000p-1f},{0x1.b6fa6e0000000p-4f,0x1.fd0d160000000p-1f},
{0x1.c3785c0000000p-4f,0x1.fce1600000000p-1f},{0x1.cff5340000000p-4f,0x1.fcb4700000000p-1f},{0x1.dc70ec0000000p-4f,0x1.fc86460000000p-1f},{0x1.e8eb800000000p-4f,0x1.fc56e40000000p-1f},
{0x1.f564e60000000p-4f,0x1.fc26480000000p-1f},{0x1.00ee8a0000000p-3f,0x1.fbf4700000000p-1f},{0x1.072a040000000p-3f,0x1.fbc1620000000p-1f},{0x1.0d64dc0000000p-3f,0x1.fb8d180000000p-1f},
{0x1.139f0c0000000p-3f,0x1.fb57980000000p-1f},{0x1.19d8940000000p-3f,0x1.fb20dc0000000p-1f},{0x1.20116e0000000p-3f,0x1.fae8e80000000p-1f},{0x1.2649940000000p-3f,0x1.faafbc0000000p-1f},
{0x1.2c81060000000p-3f,0x1.fa75580000000p-1f},{0x1.32b7c00000000p-3f,0x1.fa39ba0000000p-1f},{0x1.38edbc0000000p-3f,0x1.f9fce60000000p-1f},{0x1.3f22f60000000p-3f,0x1.f9bed80000000p-1f},
{0x1.45576c0000000p-3f,0x1.f97f920000000p-1f},{0x1.4b8b180000000p-3f,0x1.f93f140000000p-1f},{0x1.51bdf80000000p-3f,0x1.f8fd600000000p-1f},{0x1.57f0080000000p-3f,0x1.f8ba740000000p-1f},
{0x1.5e21440000000p-3f,0x1.f876500000000p-1f},{0x1.6451a80000000p-3f,0x1.f830f40000000p-1f},{0x1.6a81300000000p-3f,0x1.f7ea620000000p-1f},{0x1.70afd80000000p-3f,0x1.f7a29a0000000p-1f},
{0x1.76dd9e0000000p-3f,0x1.f7599a0000000p-1f},{0x1.7d0a7c0000000p-3f,0x1.f70f640000000p-1f},{0x1.83366e0000000p-3f,0x1.f6c3f80000000p-1f},{0x1.8961720000000p-3f,0x1.f677560000000p-1f},
{0x1.8f8b840000000p-3f,0x1.f6297c0000000p-1f},{0x1.95b49e0000000p-3f,0x1.f5da6e0000000p-1f},{0x1.9bdcc00000000p-3f,0x1.f58a2c0000000p-1f},{0x1.a203e20000000p-3f,0x1.f538b20000000p-1f},
{0x1.a82a020000000p-3f,0x1.f4e6040000000p-1f},{0x1.ae4f1e0000000p-3f,0x1.f492200000000p-1f},{0x1.b4732e0000000p-3f,0x1.f43d080000000p-1f},{0x1.ba96340000000p-3f,0x1.f3e6bc0000000p-1f},
{0x1.c0b8260000000p-3f,0x1.f38f3a0000000p-1f},{0x1.c6d9060000000p-3f,0x1.f336860000000p-1f},{0x1.ccf8cc0000000p-3f,0x1.f2dc9c0000000p-1f},{0x1.d317740000000p-3f,0x1.f281800000000p-1f},
{0x1.d934fe0000000p-3f,0x1.f225300000000p-1f},{0x1.df51640000000p-3f,0x1.f1c7ac0000000p-1f},{0x1.e56ca20000000p-3f,0x1.f168f60000000p-1f},{0x1.eb86b40000000p-3f,0x1.f1090c0000000p-1f},
{0x1.f19f980000000p-3f,0x1.f0a7f00000000p-1f},{0x1.f7b7480000000p-3f,0x1.f045a20000000p-1f},{0x1.fdcdc20000000p-3f,0x1.efe2200000000p-1f},{0x1.01f1800000000p-2f,0x1.ef7d6e0000000p-1f},
{0x1.04fb800000000p-2f,0x1.ef178a0000000p-1f},{0x1.0804e00000000p-2f,0x1.eeb0740000000p-1f},{0x1.0b0d9c0000000p-2f,0x1.ee482e0000000p-1f},{0x1.0e15b40000000p-2f,0x1.eddeb60000000p-1f},
{0x1.111d260000000p-2f,0x1.ed740e0000000p-1f},{0x1.1423ee0000000p-2f,0x1.ed08360000000p-1f},{0x1.172a0e0000000p-2f,0x1.ec9b2e0000000p-1f},{0x1.1a2f800000000p-2f,0x1.ec2cf40000000p-1f},
{0x1.1d34440000000p-2f,0x1.ebbd8c0000000p-1f},{0x1.2038580000000p-2f,0x1.eb4cf60000000p-1f},{0x1.233bba0000000p-2f,0x1.eadb2e0000000p-1f},{0x1.263e6a0000000p-2f,0x1.ea683a0000000p-1f},
{0x1.2940620000000p-2f,0x1.e9f4160000000p-1f},{0x1.2c41a40000000p-2f,0x1.e97ec40000000p-1f},{0x1.2f422e0000000p-2f,0x1.e908440000000p-1f},{0x1.3241fc0000000p-2f,0x1.e890960000000p-1f},
{0x1.35410c0000000p-2f,0x1.e817ba0000000p-1f},{0x1.383f5e0000000p-2f,0x1.e79db20000000p-1f},{0x1.3b3cf00000000p-2f,0x1.e7227e0000000p-1f},{0x1.3e39be0000000p-2f,0x1.e6a61c0000000p-1f},
{0x1.4135ca0000000p-2f,0x1.e6288e0000000p-1f},{0x1.44310e0000000p-2f,0x1.e5a9d60000000p-1f},{0x1.472b8a0000000p-2f,0x1.e529f00000000p-1f},{0x1.4a253e0000000p-2f,0x1.e4a8e00000000p-1f},
{0x1.4d1e240000000p-2f,0x1.e426a40000000p-1f},{0x1.50163e0000000p-2f,0x1.e3a33e0000000p-1f},{0x1.530d880000000p-2f,0x1.e31eae0000000p-1f},{0x1.5604020000000p-2f,0x1.e298f40000000p-1f},
{0x1.58f9a80000000p-2f,0x1.e212100000000p-1f},{0x1.5bee780000000p-2f,0x1.e18a020000000p-1f},{0x1.5ee2740000000p-2f,0x1.e100cc0000000p-1f},{0x1.61d5960000000p-2f,0x1.e0766e0000000p-1f},
{0x1.64c7de0000000p-2f,0x1.dfeae60000000p-1f},{0x1.67b94a0000000p-2f,0x1.df5e360000000p-1f},{0x1.6aa9d80000000p-2f,0x1.ded0600000000p-1f},{0x1.6d99860000000p-2f,0x1.de41600000000p-1f},
{0x1.7088540000000p-2f,0x1.ddb13c0000000p-1f},{0x1.73763c0000000p-2f,0x1.dd1ff00000000p-1f},{0x1.7663400000000p-2f,0x1.dc8d7c0000000p-1f},{0x1.794f5e0000000p-2f,0x1.dbf9e40000000p-1f},
{0x1.7c3a940000000p-2f,0x1.db65260000000p-1f},{0x1.7f24de0000000p-2f,0x1.dacf420000000p-1f},{0x1.820e3c0000000p-2f,0x1.da383a0000000p-1f},{0x1.84f6aa0000000p-2f,0x1.d9a00e0000000p-1f},
{0x1.87de2a0000000p-2f,0x1.d906bc0000000p-1f},{0x1.8ac4b80000000p-2f,0x1.d86c480000000p-1f},{0x1.8daa520000000p-2f,0x1.d7d0b00000000p-1f},{0x1.908ef80000000p-2f,0x1.d733f60000000p-1f},
{0x1.9372a60000000p-2f,0x1.d696180000000p-1f},{0x1.96555c0000000p-2f,0x1.d5f7180000000p-1f},{0x1.9937160000000p-2f,0x1.d556f60000000p-1f},{0x1.9c17d40000000p-2f,0x1.d4b5b20000000p-1f},
{0x1.9ef7940000000p-2f,0x1.d4134e0000000p-1f},{0x1.a1d6540000000p-2f,0x1.d36fc80000000p-1f},{0x1.a4b4120000000p-2f,0x1.d2cb220000000p-1f},{0x1.a790ce0000000p-2f,0x1.d2255c0000000p-1f},
{0x1.aa6c820000000p-2f,0x1.d17e780000000p-1f},{0x1.ad47320000000p-2f,0x1.d0d6720000000p-1f},{0x1.b020d60000000p-2f,0x1.d02d500000000p-1f},{0x1.b2f9720000000p-2f,0x1.cf830e0000000p-1f},
{0x1.b5d1000000000p-2f,0x1.ced7b00000000p-1f},{0x1.b8a7820000000p-2f,0x1.ce2b320000000p-1f},{0x1.bb7cf20000000p-2f,0x1.cd7d980000000p-1f},{0x1.be51520000000p-2f,0x1.cccee20000000p-1f},
{0x1.c1249e0000000p-2f,0x1.cc1f100000000p-1f},{0x1.c3f6d40000000p-2f,0x1.cb6e200000000p-1f},{0x1.c6c7f40000000p-2f,0x1.cabc160000000p-1f},{0x1.c997fc0000000p-2f,0x1.ca08f20000000p-1f},
{0x1.cc66ea0000000p-2f,0x1.c954b20000000p-1f},{0x1.cf34ba0000000p-2f,0x1.c89f580000000p-1f},{0x1.d2016e0000000p-2f,0x1.c7e8e60000000p-1f},{0x1.d4cd020000000p-2f,0x1.c731580000000p-1f},
{0x1.d797760000000p-2f,0x1.c678b40000000p-1f},{0x1.da60c60000000p-2f,0x1.c5bef60000000p-1f},{0x1.dd28f20000000p-2f,0x1.c504200000000p-1f},{0x1.dfeff60000000p-2f,0x1.c448340000000p-1f},
{0x1.e2b5d40000000p-2f,0x1.c38b300000000p-1f},{0x1.e57a860000000p-2f,0x1.c2cd140000000p-1f},{0x1.e83e0e0000000p-2f,0x1.c20de40000000p-1f},{0x1.eb006a0000000p-2f,0x1.c14d9e0000000p-1f},
{0x1.edc1960000000p-2f,0x1.c08c420000000p-1f},{0x1.f081900000000p-2f,0x1.bfc9d20000000p-1f},{0x1.f3405a0000000p-2f,0x1.bf064e0000000p-1f},{0x1.f5fdee0000000p-2f,0x1.be41b60000000p-1f},
{0x1.f8ba4e0000000p-2f,0x1.bd7c0a0000000p-1f},{0x1.fb75760000000p-2f,0x1.bcb54c0000000p-1f},{0x1.fe2f640000000p-2f,0x1.bbed7c0000000p-1f},{0x1.00740c0000000p-1f,0x1.bb249a0000000p-1f},
{0x1.01cfc80000000p-1f,0x1.ba5aa60000000p-1f},{0x1.032ae60000000p-1f,0x1.b98fa20000000p-1f},{0x1.0485620000000p-1f,0x1.b8c38e0000000p-1f},{0x1.05df3e0000000p-1f,0x1.b7f6680000000p-1f},
{0x1.07387a0000000p-1f,0x1.b728340000000p-1f},{0x1.0891120000000p-1f,0x1.b658f20000000p-1f},{0x1.09e9080000000p-1f,0x1.b588a00000000p-1f},{0x1.0b40580000000p-1f,0x1.b4b7400000000p-1f},
{0x1.0c97040000000p-1f,0x1.b3e4d40000000p-1f},{0x1.0ded0c0000000p-1f,0x1.b3115a0000000p-1f},{0x1.0f426c0000000p-1f,0x1.b23cd40000000p-1f},{0x1.1097240000000p-1f,0x1.b167420000000p-1f},
{0x1.11eb360000000p-1f,0x1.b090a60000000p-1f},{0x1.133e9c0000000p-1f,0x1.afb8fe0000000p-1f},{0x1.14915a0000000p-1f,0x1.aee04c0000000p-1f},{0x1.15e36e0000000p-1f,0x1.ae06900000000p-1f},
{0x1.1734d60000000p-1f,0x1.ad2bca0000000p-1f},{0x1.1885920000000p-1f,0x1.ac4ffc0000000p-1f},{0x1.19d5a00000000p-1f,0x1.ab73260000000p-1f},{0x1.1b25020000000p-1f,0x1.aa95480000000p-1f},
{0x1.1c73b40000000p-1f,0x1.a9b6620000000p-1f},{0x1.1dc1b60000000p-1f,0x1.a8d6760000000p-1f},{0x1.1f0f080000000p-1f,0x1.a7f5860000000p-1f},{0x1.205baa0000000p-1f,0x1.a7138e0000000p-1f},
{0x1.21a79a0000000p-1f,0x1.a630920000000p-1f},{0x1.22f2d60000000p-1f,0x1.a54c920000000p-1f},{0x1.243d600000000p-1f,0x1.a4678c0000000p-1f},{0x1.2587340000000p-1f,0x1.a381840000000p-1f},
{0x1.26d0540000000p-1f,0x1.a29a7a0000000p-1f},{0x1.2818be0000000p-1f,0x1.a1b26e0000000p-1f},{0x1.2960720000000p-1f,0x1.a0c95e0000000p-1f},{0x1.2aa76e0000000p-1f,0x1.9fdf500000000p-1f},
{0x1.2bedb20000000p-1f,0x1.9ef43e0000000p-1f},{0x1.2d333e0000000p-1f,0x1.9e082e0000000p-1f},{0x1.2e780e0000000p-1f,0x1.9d1b200000000p-1f},{0x1.2fbc240000000p-1f,0x1.9c2d120000000p-1f},
{0x1.30ff800000000p-1f,0x1.9b3e040000000p-1f},{0x1.32421e0000000p-1f,0x1.9a4dfa0000000p-1f},{0x1.3384000000000p-1f,0x1.995cf20000000p-1f},{0x1.34c5260000000p-1f,0x1.986af00000000p-1f},
{0x1.36058c0000000p-1f,0x1.9777f00000000p-1f},{0x1.3745320000000p-1f,0x1.9683f40000000p-1f},{0x1.3884180000000p-1f,0x1.958efe0000000p-1f},{0x1.39c23e0000000p-1f,0x1.94990e0000000p-1f},
{0x1.3affa20000000p-1f,0x1.93a2240000000p-1f},{0x1.3c3c440000000p-1f,0x1.92aa420000000p-1f},{0x1.3d78240000000p-1f,0x1.91b1660000000p-1f},{0x1.3eb33e0000000p-1f,0x1.90b7940000000p-1f},
{0x1.3fed960000000p-1f,0x1.8fbcca0000000p-1f},{0x1.4127260000000p-1f,0x1.8ec10a0000000p-1f},{0x1.425ff20000000p-1f,0x1.8dc4540000000p-1f},{0x1.4397f60000000p-1f,0x1.8cc6a80000000p-1f},
{0x1.44cf320000000p-1f,0x1.8bc8060000000p-1f},{0x1.4605a60000000p-1f,0x1.8ac8720000000p-1f},{0x1.473b520000000p-1f,0x1.89c7ea0000000p-1f},{0x1.4870340000000p-1f,0x1.88c66e0000000p-1f},
{0x1.49a44a0000000p-1f,0x1.87c4000000000p-1f},{0x1.4ad7960000000p-1f,0x1.86c0a20000000p-1f},{0x1.4c0a140000000p-1f,0x1.85bc520000000p-1f},{0x1.4d3bc60000000p-1f,0x1.84b7120000000p-1f},
{0x1.4e6cac0000000p-1f,0x1.83b0e00000000p-1f},{0x1.4f9cc20000000p-1f,0x1.82a9c20000000p-1f},{0x1.50cc0a0000000p-1f,0x1.81a1b40000000p-1f},{0x1.51fa820000000p-1f,0x1.8098b80000000p-1f},
{0x1.53282a0000000p-1f,0x1.7f8ece0000000p-1f},{0x1.5455000000000p-1f,0x1.7e83f80000000p-1f},{0x1.5581040000000p-1f,0x1.7d78360000000p-1f},{0x1.56ac360000000p-1f,0x1.7c6b8a0000000p-1f},
{0x1.57d6940000000p-1f,0x1.7b5df20000000p-1f},{0x1.59001e0000000p-1f,0x1.7a4f700000000p-1f},{0x1.5a28d20000000p-1f,0x1.7940060000000p-1f},{0x1.5b50b20000000p-1f,0x1.782fb20000000p-1f},
{0x1.5c77bc0000000p-1f,0x1.771e760000000p-1f},{0x1.5d9dee0000000p-1f,0x1.760c520000000p-1f},{0x1.5ec34a0000000p-1f,0x1.74f9480000000p-1f},{0x1.5fe7cc0000000p-1f,0x1.73e5580000000p-1f},
{0x1.610b760000000p-1f,0x1.72d0840000000p-1f},{0x1.622e440000000p-1f,0x1.71baca0000000p-1f},{0x1.63503a0000000p-1f,0x1.70a42c0000000p-1f},{0x1.6471540000000p-1f,0x1.6f8caa0000000p-1f},
{0x1.6591920000000p-1f,0x1.6e74460000000p-1f},{0x1.66b0f40000000p-1f,0x1.6d5afe0000000p-1f},{0x1.67cf780000000p-1f,0x1.6c40d80000000p-1f},{0x1.68ed1e0000000p-1f,0x1.6b25ce0000000p-1f},
{0x1.6a09e60000000p-1f,0x1.6a09e60000000p-1f},{0x1.6b25ce0000000p-1f,0x1.68ed1e0000000p-1f},{0x1.6c40d80000000p-1f,0x1.67cf780000000p-1f},{0x1.6d5afe0000000p-1f,0x1.66b0f40000000p-1f},
{0x1.6e74460000000p-1f,0x1.6591920000000p-1f},{0x1.6f8caa0000000p-1f,0x1.6471540000000p-1f},{0x1.70a42c0000000p-1f,0x1.63503a0000000p-1f},{0x1.71baca0000000p-1f,0x1.622e440000000p-1f},
{0x1.72d0840000000p-1f,0x1.610b760000000p-1f},{0x1.73e5580000000p-1f,0x1.5fe7cc0000000p-1f},{0x1.74f9480000000p-1f,0x1.5ec34a0000000p-1f},{0x1.760c520000000p-1f,0x1.5d9dee0000000p-1f},
{0x1.771e760000000p-1f,0x1.5c77bc0000000p-1f},{0x1.782fb20000000p-1f,0x1.5b50b20000000p-1f},{0x1.7940060000000p-1f,0x1.5a28d20000000p-1f},{0x1.7a4f700000000p-1f,0x1.59001e0000000p-1f},
{0x1.7b5df20000000p-1f,0x1.57d6940000000p-1f},{0x1.7c6b8a0000000p-1f,0x1.56ac360000000p-1f},{0x1.7d78360000000p-1f,0x1.5581040000000p-1f},{0x1.7e83f80000000p-1f,0x1.5455000000000p-1f},
{0x1.7f8ece0000000p-1f,0x1.53282a0000000p-1f},{0x1.8098b80000000p-1f,0x1.51fa820000000p-1f},{0x1.81a1b40000000p-1f,0x1.50cc0a0000000p-1f},{0x1.82a9c20000000p-1f,0x1.4f9cc20000000p-1f},
{0x1.83b0e00000000p-1f,0x1.4e6cac0000000p-1f},{0x1.84b7120000000p-1f,0x1.4d3bc60000000p-1f},{0x1.85bc520000000p-1f,0x1.4c0a140000000p-1f},{0x1.86c0a20000000p-1f,0x1.4ad7960000000p-1f},
{0x1.87c4000000000p-1f,0x1.49a44a0000000p-1f},{0x1.88c66e0000000p-1f,0x1.4870340000000p-1f},{0x1.89c7ea0000000p-1f,0x1.473b520000000p-1f},{0x1.8ac8720000000p-1f,0x1.4605a60000000p-1f},
{0x1.8bc8060000000p-1f,0x1.44cf320000000p-1f},{0x1.8cc6a80000000p-1f,0x1.4397f60000000p-1f},{0x1.8dc4540000000p-1f,0x1.425ff20000000p-1f},{0x1.8ec10a0000000p-1f,0x1.4127260000000p-1f},
{0x1.8fbcca0000000p-1f,0x1.3fed960000000p-1f},{0x1.90b7940000000p-1f,0x1.3eb33e0000000p-1f},{0x1.91b1660000000p-1f,0x1.3d78240000000p-1f},{0x1.92aa420000000p-1f,0x1.3c3c440000000p-1f},
{0x1.93a2240000000p-1f,0x1.3affa20000000p-1f},{0x1.94990e0000000p-1f,0x1.39c23e0000000p-1f},{0x1.958efe0000000p-1f,0x1.3884180000000p-1f},{0x1.9683f40000000p-1f,0x1.3745320000000p-1f},
{0x1.9777f00000000p-1f,0x1.36058c0000000p-1f},{0x1.986af00000000p-1f,0x1.34c5260000000p-1f},{0x1.995cf20000000p-1f,0x1.3384000000000p-1f},{0x1.9a4dfa0000000p-1f,0x1.32421e0000000p-1f},
{0x1.9b3e040000000p-1f,0x1.30ff800000000p-1f},{0x1.9c2d120000000p-1f,0x1.2fbc240000000p-1f},{0x1.9d1b200000000p-1f,0x1.2e780e0000000p-1f},{0x1.9e082e0000000p-1f,0x1.2d333e0000000p-1f},
{0x1.9ef43e0000000p-1f,0x1.2bedb20000000p-1f},{0x1.9fdf500000000p-1f,0x1.2aa76e0000000p-1f},{0x1.a0c95e0000000p-1f,0x1.2960720000000p-1f},{0x1.a1b26e0000000p-1f,0x1.2818be0000000p-1f},
{0x1.a29a7a0000000p-1f,0x1.26d0540000000p-1f},{0x1.a381840000000p-1f,0x1.2587340000000p-1f},{0x1.a4678c0000000p-1f,0x1.243d600000000p-1f},{0x1.a54c920000000p-1f,0x1.22f2d60000000p-1f},
{0x1.a630920000000p-1f,0x1.21a79a0000000p-1f},{0x1.a7138e0000000p-1f,0x1.205baa0000000p-1f},{0x1.a7f5860000000p-1f,0x1.1f0f080000000p-1f},{0x1.a8d6760000000p-1f,0x1.1dc1b60000000p-1f},
{0x1.a9b6620000000p-1f,0x1.1c73b40000000p-1f},{0x1.aa95480000000p-1f,0x1.1b25020000000p-1f},{0x1.ab73260000000p-1f,0x1.19d5a00000000p-1f},{0x1.ac4ffc0000000p-1f,0x1.1885920000000p-1f},
{0x1.ad2bca0000000p-1f,0x1.1734d60000000p-1f},{0x1.ae06900000000p-1f,0x1.15e36e0000000p-1f},{0x1.aee04c0000000p-1f,0x1.14915a0000000p-1f},{0x1.afb8fe0000000p-1f,0x1.133e9c0000000p-1f},
{0x1.b090a60000000p-1f,0x1.11eb360000000p-1f},{0x1.b167420000000p-1f,0x1.1097240000000p-1f},{0x1.b23cd40000000p-1f,0x1.0f426c0000000p-1f},{0x1.b3115a0000000p-1f,0x1.0ded0c0000000p-1f},
{0x1.b3e4d40000000p-1f,0x1.0c97040000000p-1f},{0x1.b4b7400000000p-1f,0x1.0b40580000000p-1f},{0x1.b588a00000000p-1f,0x1.09e9080000000p-1f},{0x1.b658f20000000p-1f,0x1.0891120000000p-1f},
{0x1.b728340000000p-1f,0x1.07387a0000000p-1f},{0x1.b7f6680000000p-1f,0x1.05df3e0000000p-1f},{0x1.b8c38e0000000p-1f,0x1.0485620000000p-1f},{0x1.b98fa20000000p-1f,0x1.032ae60000000p-1f},
{0x1.ba5aa60000000p-1f,0x1.01cfc80000000p-1f},{0x1.bb249a0000000p-1f,0x1.00740c0000000p-1f},{0x1.bbed7c0000000p-1f,0x1.fe2f640000000p-2f},{0x1.bcb54c0000000p-1f,0x1.fb75760000000p-2f},
{0x1.bd7c0a0000000p-1f,0x1.f8ba4e0000000p-2f},{0x1.be41b60000000p-1f,0x1.f5fdee0000000p-2f},{0x1.bf064e0000000p-1f,0x1.f3405a0000000p-2f},{0x1.bfc9d20000000p-1f,0x1.f081900000000p-2f},
{0x1.c08c420000000p-1f,0x1.edc1960000000p-2f},{0x1.c14d9e0000000p-1f,0x1.eb006a0000000p-2f},{0x1.c20de40000000p-1f,0x1.e83e0e0000000p-2f},{0x1.c2cd140000000p-1f,0x1.e57a860000000p-2f},
{0x1.c38b300000000p-1f,0x1.e2b5d40000000p-2f},{0x1.c448340000000p-1f,0x1.dfeff60000000p-2f},{0x1.c504200000000p-1f,0x1.dd28f20000000p-2f},{0x1.c5bef60000000p-1f,0x1.da60c60000000p-2f},
{0x1.c678b40000000p-1f,0x1.d797760000000p-2f},{0x1.c731580000000p-1f,0x1.d4cd020000000p-2f},{0x1.c7e8e60000000p-1f,0x1.d2016e0000000p-2f},{0x1.c89f580000000p-1f,0x1.cf34ba0000000p-2f},
{0x1.c954b20000000p-1f,0x1.cc66ea0000000p-2f},{0x1.ca08f20000000p-1f,0x1.c997fc0000000p-2f},{0x1.cabc160000000p-1f,0x1.c6c7f40000000p-2f},{0x1.cb6e200000000p-1f,0x1.c3f6d40000000p-2f},
{0x1.cc1f100000000p-1f,0x1.c1249e0000000p-2f},{0x1.cccee20000000p-1f,0x1.be51520000000p-2f},{0x1.cd7d980000000p-1f,0x1.bb7cf20000000p-2f},{0x1.ce2b320000000p-1f,0x1.b8a7820000000p-2f},
{0x1.ced7b00000000p-1f,0x1.b5d1000000000p-2f},{0x1.cf830e0000000p-1f,0x1.b2f9720000000p-2f},{0x1.d02d500000000p-1f,0x1.b020d60000000p-2f},{0x1.d0d6720000000p-1f,0x1.ad47320000000p-2f},
{0x1.d17e780000000p-1f,0x1.aa6c820000000p-2f},{0x1.d2255c0000000p-1f,0x1.a790ce0000000p-2f},{0x1.d2cb220000000p-1f,0x1.a4b4120000000p-2f},{0x1.d36fc80000000p-1f,0x1.a1d6540000000p-2f},
{0x1.d4134e0000000p-1f,0x1.9ef7940000000p-2f},{0x1.d4b5b20000000p-1f,0x1.9c17d40000000p-2f},{0x1.d556f60000000p-1f,0x1.9937160000000p-2f},{0x1.d5f7180000000p-1f,0x1.96555c0000000p-2f},
{0x1.d696180000000p-1f,0x1.9372a60000000p-2f},{0x1.d733f60000000p-1f,0x1.908ef80000000p-2f},{0x1.d7d0b00000000p-1f,0x1.8daa520000000p-2f},{0x1.d86c480000000p-1f,0x1.8ac4b80000000p-2f},
{0x1.d906bc0000000p-1f,0x1.87de2a0000000p-2f},{0x1.d9a00e0000000p-1f,0x1.84f6aa0000000p-2f},{0x1.da383a0000000p-1f,0x1.820e3c0000000p-2f},{0x1.dacf420000000p-1f,0x1.7f24de0000000p-2f},
{0x1.db65260000000p-1f,0x1.7c3a940000000p-2f},{0x1.dbf9e40000000p-1f,0x1.794f5e0000000p-2f},{0x1.dc8d7c0000000p-1f,0x1.7663400000000p-2f},{0x1.dd1ff00000000p-1f,0x1.73763c0000000p-2f},
{0x1.ddb13c0000000p-1f,0x1.7088540000000p-2f},{0x1.de41600000000p-1f,0x1.6d99860000000p-2f},{0x1.ded0600000000p-1f,0x1.6aa9d80000000p-2f},{0x1.df5e360000000p-1f,0x1.67b94a0000000p-2f},
{0x1.dfeae60000000p-1f,0x1.64c7de0000000p-2f},{0x1.e0766e0000000p-1f,0x1.61d5960000000p-2f},{0x1.e100cc0000000p-1f,0x1.5ee2740000000p-2f},{0x1.e18a020000000p-1f,0x1.5bee780000000p-2f},
{0x1.e212100000000p-1f,0x1.58f9a80000000p-2f},{0x1.e298f40000000p-1f,0x1.5604020000000p-2f},{0x1.e31eae0000000p-1f,0x1.530d880000000p-2f},{0x1.e3a33e0000000p-1f,0x1.50163e0000000p-2f},
{0x1.e426a40000000p-1f,0x1.4d1e240000000p-2f},{0x1.e4a8e00000000p-1f,0x1.4a253e0000000p-2f},{0x1.e529f00000000p-1f,0x1.472b8a0000000p-2f},{0x1.e5a9d60000000p-1f,0x1.44310e0000000p-2f},
{0x1.e6288e0000000p-1f,0x1.4135ca0000000p-2f},{0x1.e6a61c0000000p-1f,0x1.3e39be0000000p-2f},{0x1.e7227e0000000p-1f,0x1.3b3cf00000000p-2f},{0x1.e79db20000000p-1f,0x1.383f5e0000000p-2f},
{0x1.e817ba0000000p-1f,0x1.35410c0000000p-2f},{0x1.e890960000000p-1f,0x1.3241fc0000000p-2f},{0x1.e908440000000p-1f,0x1.2f422e0000000p-2f},{0x1.e97ec40000000p-1f,0x1.2c41a40000000p-2f},
{0x1.e9f4160000000p-1f,0x1.2940620000000p-2f},{0x1.ea683a0000000p-1f,0x1.263e6a0000000p-2f},{0x1.eadb2e0000000p-1f,0x1.233bba0000000p-2f},{0x1.eb4cf60000000p-1f,0x1.2038580000000p-2f},
{0x1.ebbd8c0000000p-1f,0x1.1d34440000000p-2f},{0x1.ec2cf40000000p-1f,0x1.1a2f800000000p-2f},{0x1.ec9b2e0000000p-1f,0x1.172a0e0000000p-2f},{0x1.ed08360000000p-1f,0x1.1423ee0000000p-2f},
{0x1.ed740e0000000p-1f,0x1.111d260000000p-2f},{0x1.eddeb60000000p-1f,0x1.0e15b40000000p-2f},{0x1.ee482e0000000p-1f,0x1.0b0d9c0000000p-2f},{0x1.eeb0740000000p-1f,0x1.0804e00000000p-2f},
{0x1.ef178a0000000p-1f,0x1.04fb800000000p-2f},{0x1.ef7d6e0000000p-1f,0x1.01f1800000000p-2f},{0x1.efe2200000000p-1f,0x1.fdcdc20000000p-3f},{0x1.f045a20000000p-1f,0x1.f7b7480000000p-3f},
{0x1.f0a7f00000000p-1f,0x1.f19f980000000p-3f},{0x1.f1090c0000000p-1f,0x1.eb86b40000000p-3f},{0x1.f168f60000000p-1f,0x1.e56ca20000000p-3f},{0x1.f1c7ac0000000p-1f,0x1.df51640000000p-3f},
{0x1.f225300000000p-1f,0x1.d934fe0000000p-3f},{0x1.f281800000000p-1f,0x1.d317740000000p-3f},{0x1.f2dc9c0000000p-1f,0x1.ccf8cc0000000p-3f},{0x1.f336860000000p-1f,0x1.c6d9060000000p-3f},
{0x1.f38f3a0000000p-1f,0x1.c0b8260000000p-3f},{0x1.f3e6bc0000000p-1f,0x1.ba96340000000p-3f},{0x1.f43d080000000p-1f,0x1.b4732e0000000p-3f},{0x1.f492200000000p-1f,0x1.ae4f1e0000000p-3f},
{0x1.f4e6040000000p-1f,0x1.a82a020000000p-3f},{0x1.f538b20000000p-1f,0x1.a203e20000000p-3f},{0x1.f58a2c0000000p-1f,0x1.9bdcc00000000p-3f},{0x1.f5da6e0000000p-1f,0x1.95b49e0000000p-3f},
{0x1.f6297c0000000p-1f,0x1.8f8b840000000p-3f},{0x1.f677560000000p-1f,0x1.8961720000000p-3f},{0x1.f6c3f80000000p-1f,0x1.83366e0000000p-3f},{0x1.f70f640000000p-1f,0x1.7d0a7c0000000p-3f},
{0x1.f7599a0000000p-1f,0x1.76dd9e0000000p-3f},{0x1.f7a29a0000000p-1f,0x1.70afd80000000p-3f},{0x1.f7ea620000000p-1f,0x1.6a81300000000p-3f},{0x1.f830f40000000p-1f,0x1.6451a80000000p-3f},
{0x1.f876500000000p-1f,0x1.5e21440000000p-3f},{0x1.f8ba740000000p-1f,0x1.57f0080000000p-3f},{0x1.f8fd600000000p-1f,0x1.51bdf80000000p-3f},{0x1.f93f140000000p-1f,0x1.4b8b180000000p-3f},
{0x1.f97f920000000p-1f,0x1.45576c0000000p-3f},{0x1.f9bed80000000p-1f,0x1.3f22f60000000p-3f},{0x1.f9fce60000000p-1f,0x1.38edbc0000000p-3f},{0x1.fa39ba0000000p-1f,0x1.32b7c00000000p-3f},
{0x1.fa75580000000p-1f,0x1.2c81060000000p-3f},{0x1.faafbc0000000p-1f,0x1.2649940000000p-3f},{0x1.fae8e80000000p-1f,0x1.20116e0000000p-3f},{0x1.fb20dc0000000p-1f,0x1.19d8940000000p-3f},
{0x1.fb57980000000p-1f,0x1.139f0c0000000p-3f},{0x1.fb8d180000000p-1f,0x1.0d64dc0000000p-3f},{0x1.fbc1620000000p-1f,0x1.072a040000000p-3f},{0x1.fbf4700000000p-1f,0x1.00ee8a0000000p-3f},
{0x1.fc26480000000p-1f,0x1.f564e60000000p-4f},{0x1.fc56e40000000p-1f,0x1.e8eb800000000p-4f},{0x1.fc86460000000p-1f,0x1.dc70ec0000000p-4f},{0x1.fcb4700000000p-1f,0x1.cff5340000000p-4f},
{0x1.fce1600000000p-1f,0x1.c3785c0000000p-4f},{0x1.fd0d160000000p-1f,0x1.b6fa6e0000000p-4f},{0x1.fd37920000000p-1f,0x1.aa7b720000000p-4f},{0x1.fd60d20000000p-1f,0x1.9dfb6e0000000p-4f},
{0x1.fd88da0000000p-1f,0x1.917a6c0000000p-4f},{0x1.fdafa80000000p-1f,0x1.84f8720000000p-4f},{0x1.fdd53a0000000p-1f,0x1.7875860000000p-4f},{0x1.fdf9920000000p-1f,0x1.6bf1b40000000p-4f},
{0x1.fe1cb00000000p-1f,0x1.5f6d000000000p-4f},{0x1.fe3e920000000p-1f,0x1.52e7740000000p-4f},{0x1.fe5f3a0000000p-1f,0x1.4661180000000p-4f},{0x1.fe7ea80000000p-1f,0x1.39d9f20000000p-4f},
{0x1.fe9cda0000000p-1f,0x1.2d520a0000000p-4f},{0x1.feb9d20000000p-1f,0x1.20c9680000000p-4f},{0x1.fed58e0000000p-1f,0x1.1440140000000p-4f},{0x1.fef0100000000p-1f,0x1.07b6140000000p-4f},
{0x1.ff09560000000p-1f,0x1.f656e80000000p-5f},{0x1.ff21620000000p-1f,0x1.dd40700000000p-5f},{0x1.ff38300000000p-1f,0x1.c428d20000000p-5f},{0x1.ff4dc60000000p-1f,0x1.ab101c0000000p-5f},
{0x1.ff621e0000000p-1f,0x1.91f6600000000p-5f},{0x1.ff753c0000000p-1f,0x1.78dbaa0000000p-5f},{0x1.ff871e0000000p-1f,0x1.5fc00e0000000p-5f},{0x1.ff97c40000000p-1f,0x1.46a3960000000p-5f},
{0x1.ffa72e0000000p-1f,0x1.2d86580000000p-5f},{0x1.ffb55e0000000p-1f,0x1.14685e0000000p-5f},{0x1.ffc2520000000p-1f,0x1.f693740000000p-6f},{0x1.ffce0a0000000p-1f,0x1.c454f40000000p-6f},
{0x1.ffd8860000000p-1f,0x1.9215600000000p-6f},{0x1.ffe1c60000000p-1f,0x1.5fd4d20000000p-6f},{0x1.ffe9cc0000000p-1f,0x1.2d936c0000000p-6f},{0x1.fff0940000000p-1f,0x1.f6a2960000000p-7f},
{0x1.fff6220000000p-1f,0x1.921d200000000p-7f},{0x1.fffa720000000p-1f,0x1.2d96b00000000p-7f},{0x1.fffd880000000p-1f,0x1.921f100000000p-8f},{0x1.ffff620000000p-1f,0x1.921f8c0000000p-9f}
};
#define TW640 (reinterpret_cast<const cf*>(TW640_))
#define TW2048 (reinterpret_cast<const cf*>(TW2048_))

__device__ __forceinline__ float bf2f(unsigned short u) { return __uint_as_float(((unsigned)u) << 16); }
__device__ __forceinline__ unsigned short f2bf(float f) {
  __hip_bfloat16 h = __float2bfloat16(f);
  return *reinterpret_cast<unsigned short*>(&h);
}
__device__ __forceinline__ unsigned pack2(float a, float b) {
  unsigned r;
  asm("v_cvt_pk_bf16_f32 %0, %1, %2" : "=v"(r) : "v"(a), "v"(b));
  return r;
}
__device__ __forceinline__ float rbf(float f) { return __uint_as_float(pack2(0.f, f) & 0xFFFF0000u); }
__device__ __forceinline__ float lo_f(unsigned u) { return __uint_as_float(u << 16); }
__device__ __forceinline__ float hi_f(unsigned u) { return __uint_as_float(u & 0xFFFF0000u); }
__device__ __forceinline__ cf rbf2(cf v) { unsigned u = pack2(v.x, v.y); return cf{lo_f(u), hi_f(u)}; }

template <bool INV> __device__ __forceinline__ cf add_rot(cf a, cf b) {
  cf r;
  if (INV) asm("v_pk_add_f32 %0, %1, %2 op_sel:[0,1] op_sel_hi:[1,0] neg_lo:[0,1]" : "=v"(r) : "v"(a), "v"(b));
  else     asm("v_pk_add_f32 %0, %1, %2 op_sel:[0,1] op_sel_hi:[1,0] neg_hi:[0,1]" : "=v"(r) : "v"(a), "v"(b));
  return r;
}
template <bool INV> __device__ __forceinline__ cf sub_rot(cf a, cf b) { return add_rot<!INV>(a, b); }
__device__ __forceinline__ cf cmul(cf a, cf w) {
  cf t, r;
  asm("v_pk_mul_f32 %0, %1, %2 op_sel:[0,0] op_sel_hi:[0,1]" : "=v"(t) : "v"(a), "v"(w));
  asm("v_pk_fma_f32 %0, %1, %2, %3 op_sel:[1,1,0] op_sel_hi:[1,0,1] neg_lo:[0,1,0]" : "=v"(r) : "v"(a), "v"(w), "v"(t));
  return r;
}
__device__ __forceinline__ cf cmulc(cf a, cf w) {
  cf t, r;
  asm("v_pk_mul_f32 %0, %1, %2 op_sel:[0,0] op_sel_hi:[1,0]" : "=v"(t) : "v"(a), "v"(w));
  asm("v_pk_fma_f32 %0, %1, %2, %3 op_sel:[1,1,0] op_sel_hi:[0,1,1] neg_hi:[1,0,0]" : "=v"(r) : "v"(a), "v"(w), "v"(t));
  return r;
}
__device__ __forceinline__ cf pfma(cf a, cf b, cf c) { return __builtin_elementwise_fma(a, b, c); }

template <bool INV> __device__ __forceinline__ void dft4(cf& x0, cf& x1, cf& x2, cf& x3) {
  cf a0 = x0 + x2, a1 = x0 - x2, a2 = x1 + x3, d = x1 - x3;
  x0 = a0 + a2; x2 = a0 - a2; x1 = add_rot<INV>(a1, d); x3 = sub_rot<INV>(a1, d);
}

template <bool INV> __device__ __forceinline__ void dft8(cf* v) {
  const cf rr = {0.70710678118654752440f, 0.70710678118654752440f};
  const cf nr = {-0.70710678118654752440f, -0.70710678118654752440f};
  cf e0 = v[0], e1 = v[2], e2 = v[4], e3 = v[6];
  cf o0 = v[1], o1 = v[3], o2 = v[5], o3 = v[7];
  dft4<INV>(e0, e1, e2, e3);
  dft4<INV>(o0, o1, o2, o3);
  cf s1 = add_rot<INV>(o1, o1);
  cf s3 = sub_rot<INV>(o3, o3);
  v[0] = e0 + o0; v[4] = e0 - o0;
  v[1] = pfma(s1, rr, e1); v[5] = pfma(s1, nr, e1);
  v[2] = add_rot<INV>(e2, o2); v[6] = sub_rot<INV>(e2, o2);
  v[3] = pfma(s3, nr, e3); v[7] = pfma(s3, rr, e3);
}

template <bool INV> __device__ __forceinline__ void dft5(const cf* x, cf* X) {
  const cf c1 = {0.30901699437494742410f, 0.30901699437494742410f}, c2 = {-0.80901699437494742410f, -0.80901699437494742410f};
  const cf s1 = {0.95105651629515357212f, 0.95105651629515357212f}, s2 = {0.58778525229247312917f, 0.58778525229247312917f};
  cf t1 = x[1] + x[4], t2 = x[2] + x[3], t3 = x[1] - x[4], t4 = x[2] - x[3];
  X[0] = x[0] + t1 + t2;
  cf m1 = pfma(c2, t2, pfma(c1, t1, x[0]));
  cf m2 = pfma(c1, t2, pfma(c2, t1, x[0]));
  cf n1 = pfma(s2, t4, s1 * t3);
  cf n2 = pfma(-s1, t4, s2 * t3);
  X[1] = add_rot<INV>(m1, n1); X[4] = sub_rot<INV>(m1, n1);
  X[2] = add_rot<INV>(m2, n2); X[3] = sub_rot<INV>(m2, n2);
}

template <bool INV> __device__ __forceinline__ void dft10(cf* v) {
  cf u[5], d[5], U[5], Dd[5];
#pragma unroll
  for (int n2 = 0; n2 < 5; ++n2) {
    cf a = v[(2 * n2) % 10], b = v[(5 + 2 * n2) % 10];
    u[n2] = a + b; d[n2] = a - b;
  }
  dft5<INV>(u, U); dft5<INV>(d, Dd);
#pragma unroll
  for (int k2 = 0; k2 < 5; ++k2) {
    v[(6 * k2) % 10] = U[k2];
    v[(5 + 6 * k2) % 10] = Dd[k2];
  }
}

template <int CTRL> __device__ __forceinline__ float dppf(float v) {
  return __int_as_float(__builtin_amdgcn_update_dpp(0, __float_as_int(v), CTRL, 0xF, 0xF, true));
}
__device__ __forceinline__ float seg16_sum(float v) {
  v += dppf<0xB1>(v); v += dppf<0x4E>(v); v += dppf<0x141>(v); v += dppf<0x140>(v);
  return v;
}
__device__ __forceinline__ void tw_powers(cf w, cf* tw) {
  tw[1] = w; tw[2] = cmul(w, w); tw[3] = cmul(tw[2], w); tw[4] = cmul(tw[2], tw[2]);
  tw[5] = cmul(tw[4], w); tw[6] = cmul(tw[4], tw[2]); tw[7] = cmul(tw[4], tw[3]);
}
__device__ __forceinline__ cf fdiv2(cf a, cf d, cf rd) {
  cf q = a * rd;
  cf r = pfma(-q, d, a);
  return pfma(r, rd, q);
}
#define WAVE_SYNC() asm volatile("s_waitcnt lgkmcnt(0)" ::: "memory")

constexpr size_t WS_F   = 0;
constexpr size_t WS_WT  = WS_F + (size_t)DF * S * 8;
constexpr size_t WS_X1  = WS_WT + (size_t)DF * S * 4;
constexpr size_t WS_X2  = WS_X1 + (size_t)S * D * 2;
constexpr size_t WS_XN  = WS_X2 + (size_t)S * D * 2;
constexpr size_t WS_H   = WS_XN + (size_t)S * D * 2;
constexpr size_t WS_W1  = WS_H + (size_t)S * FF * 2;
constexpr size_t WS_W2  = WS_W1 + (size_t)D * FF * 2;
constexpr size_t WS_END = WS_W2 + (size_t)D * FF * 2;

constexpr int NF = 4;
constexpr int RT = 80 * NF;
constexpr int K1_FFT_BLOCKS = S / (2 * NF);
constexpr int PREP_W_BLOCKS = 32 * 6;
constexpr int PREP_W1_BLOCKS = 20 * 7;
constexpr int PREP_W2_BLOCKS = 54 * 2;
constexpr int PREP_BLOCKS = PREP_W_BLOCKS + PREP_W1_BLOCKS + PREP_W2_BLOCKS;
constexpr int MP = 106;
constexpr int LP = 8 * MP;

__device__ __forceinline__ void ln_finish(const cf* red, cf& mr, cf& d, cf& rd) {
  cf mu[5], m2[5];
#pragma unroll
  for (int i = 0; i < 5; ++i) { mu[i] = red[2 * i]; m2[i] = red[2 * i + 1]; }
  cf mean = ((mu[0] + mu[1]) + (mu[2] + mu[3]) + mu[4]) * 0.2f;
  cf M2 = (m2[0] + m2[1]) + (m2[2] + m2[3]) + m2[4];
  cf q = {0.f, 0.f};
#pragma unroll
  for (int i = 0; i < 5; ++i) { cf dd = mu[i] - mean; q = pfma(dd, dd, q); }
  cf var = pfma(q, cf{128.0f / D, 128.0f / D}, M2 * (1.0f / D));
  mr = rbf2(mean);
  cf e = rbf2(rbf2(var) + 1e-5f);
  d = rbf2(cf{sqrtf(e.x), sqrtf(e.y)});
  rd = cf{__builtin_amdgcn_rcpf(d.x), __builtin_amdgcn_rcpf(d.y)};
}
__device__ __forceinline__ void seg_stats(const cf* a, cf& mu, cf& m2) {
  cf s = ((a[0] + a[1]) + (a[2] + a[3])) + ((a[4] + a[5]) + (a[6] + a[7]));
  mu = cf{seg16_sum(s.x), seg16_sum(s.y)} * (1.0f / 128.0f);
  cf q = {0.f, 0.f};
#pragma unroll
  for (int i = 0; i < 8; ++i) { cf dd = a[i] - mu; q = pfma(dd, dd, q); }
  m2 = cf{seg16_sum(q.x), seg16_sum(q.y)};
}

template <int NT> __device__ __forceinline__ void prep_blocks(int b, int tid, char* lds, const unsigned short* __restrict__ wr,
                                            const unsigned short* __restrict__ wi, const unsigned short* __restrict__ w1,
                                            const unsigned short* __restrict__ w2, unsigned* __restrict__ WT,
                                            u32x4* __restrict__ w1p, u32x4* __restrict__ w2p) {
  if (b < PREP_W1_BLOCKS) {
    unsigned short* tile = reinterpret_cast<unsigned short*>(lds);
    int ks = b / 7, cb = b % 7;
    u32x4 ld[4];
#pragma unroll
    for (int i = 0; i < 4; ++i) {
      int c = tid + i * NT; c = c < 1024 ? c : 1023;
      int r = c >> 5, c8 = c & 31, f = cb * 256 + c8 * 8; f = f < FF ? f : FF - 8;
      ld[i] = *reinterpret_cast<const u32x4*>(w1 + (size_t)(32 * ks + r) * FF + f);
    }
#pragma unroll
    for (int i = 0; i < 4; ++i) {
      int c = tid + i * NT, r = c >> 5, c8 = c & 31, f = cb * 256 + c8 * 8;
      if (c < 1024 && f < FF) *reinterpret_cast<u32x4*>(tile + r * 264 + c8 * 8) = ld[i];
    }
    __syncthreads();
    for (int o = tid; o < 1024; o += NT) {
      int ftl = o >> 6, lane = o & 63, ft = cb * 16 + ftl;
      if (ft < 108) {
        const unsigned short* p = tile + (8 * (lane >> 4)) * 264 + 16 * ftl + (lane & 15);
        u32x4 v;
#pragma unroll
        for (int j = 0; j < 4; ++j) v[j] = (unsigned)p[(2 * j) * 264] | ((unsigned)p[(2 * j + 1) * 264] << 16);
        w1p[((size_t)ks * 108 + ft) * 64 + lane] = v;
      }
    }
    return;
  }
  b -= PREP_W1_BLOCKS;
  if (b < PREP_W2_BLOCKS) {
    unsigned short* tile = reinterpret_cast<unsigned short*>(lds);
    int ks = b >> 1, cb = b & 1;
    u32x4 ld[5];
#pragma unroll
    for (int i = 0; i < 5; ++i) {
      int c = tid + i * NT; c = c < 1280 ? c : 1279;
      int r = c / 40, c8 = c % 40;
      ld[i] = *reinterpret_cast<const u32x4*>(w2 + (size_t)(32 * ks + r) * D + cb * 320 + c8 * 8);
    }
#pragma unroll
    for (int i = 0; i < 5; ++i) {
      int c = tid + i * NT, r = c / 40, c8 = c % 40;
      if (c < 1280) *reinterpret_cast<u32x4*>(tile + r * 328 + c8 * 8) = ld[i];
    }
    __syncthreads();
    for (int o = tid; o < 1280; o += NT) {
      int ntl = o >> 6, lane = o & 63, g = lane >> 4;
      const unsigned short* p = tile + (4 * g) * 328 + 16 * ntl + (lane & 15);
      u32x4 v;
      v[0] = (unsigned)p[0] | ((unsigned)p[328] << 16);
      v[1] = (unsigned)p[2 * 328] | ((unsigned)p[3 * 328] << 16);
      v[2] = (unsigned)p[16 * 328] | ((unsigned)p[17 * 328] << 16);
      v[3] = (unsigned)p[18 * 328] | ((unsigned)p[19 * 328] << 16);
      w2p[((size_t)ks * 40 + cb * 20 + ntl) * 64 + lane] = v;
    }
    return;
  }
  b -= PREP_W2_BLOCKS;
  {
    unsigned* tile = reinterpret_cast<unsigned*>(lds);
    int pt = b / 6, kt = b % 6;
    constexpr int NI = (4096 + NT - 1) / NT;
    unsigned short lr[NI], li[NI];
#pragma unroll
    for (int i = 0; i < NI; ++i) {
      int e = tid + i * NT; e = e < 4096 ? e : 4095;
      int pl = e >> 6, kl = e & 63;
      int pos = pt * 64 + pl, kc = kt * 64 + kl;
      int m = pos >> 8, m2 = (pos >> 5) & 7, m3 = (pos >> 2) & 7, m4 = pos & 3;
      int sp = m + 8 * m2 + 64 * m3 + 512 * m4;
      kc = kc < DF ? kc : DF - 1;
      lr[i] = wr[sp * DF + kc]; li[i] = wi[sp * DF + kc];
    }
#pragma unroll
    for (int i = 0; i < NI; ++i) {
      int e = tid + i * NT;
      int pl = e >> 6, kl = e & 63;
      if (e < 4096) tile[kl * 65 + pl] = (unsigned)lr[i] | ((unsigned)li[i] << 16);
    }
    __syncthreads();
    for (int e = tid; e < 4096; e += NT) {
      int kl = e >> 6, pl = e & 63;
      int kc = kt * 64 + kl;
      if (kc < DF) WT[kc * S + pt * 64 + pl] = tile[kl * 65 + pl];
    }
  }
}

__global__ __launch_bounds__(RT) void k_rowfwd(const unsigned short* __restrict__ x, cf* __restrict__ F,
                                               const unsigned short* __restrict__ wr, const unsigned short* __restrict__ wi,
                                               const unsigned short* __restrict__ w1, const unsigned short* __restrict__ w2,
                                               unsigned* __restrict__ WT, u32x4* __restrict__ w1p, u32x4* __restrict__ w2p) {
  __shared__ __attribute__((aligned(16))) cf buf[NF * LP];
  __shared__ __attribute__((aligned(16))) cf buf2[NF * 648];
  __shared__ __attribute__((aligned(16))) cf red[NF * 10];
  const int tid = threadIdx.x;
  int b = blockIdx.x;
  if (gridDim.x > K1_FFT_BLOCKS) {
    if (b < PREP_W_BLOCKS) {
      int pb = b + PREP_W1_BLOCKS + PREP_W2_BLOCKS;
      prep_blocks<RT>(pb, tid, reinterpret_cast<char*>(buf), wr, wi, w1, w2, WT, w1p, w2p);
      return;
    }
    b -= PREP_W_BLOCKS;
  }
  const int f = tid / 80, t = tid % 80;
  const int s0 = ((b & 7) * (K1_FFT_BLOCKS / 8) + (b >> 3)) * 2 * NF;
  cf v[10];
  {
    const unsigned short* pa = x + (s0 + 2 * f) * D + t;
#pragma unroll
    for (int j = 0; j < 8; ++j) v[j] = cf{bf2f(pa[80 * j]), bf2f(pa[D + 80 * j])};
  }
  const int m_ = t / 10, u_ = t % 10;
  cf w1b = TW640[t], w2b = TW640[8 * u_];
  cf mu, m2;
  seg_stats(v, mu, m2);
  if ((t & 15) == 0) { red[f * 10 + (t >> 4) * 2] = mu; red[f * 10 + (t >> 4) * 2 + 1] = m2; }
  cf tw[8];
  tw_powers(w1b, tw);
  __syncthreads();
  cf mr, dd, rd;
  ln_finish(red + f * 10, mr, dd, rd);
#pragma unroll
  for (int j = 0; j < 8; ++j) v[j] = rbf2(fdiv2(rbf2(v[j] - mr), dd, rd));
  cf* Z = buf + f * LP;
  dft8<false>(v);
#pragma unroll
  for (int m = 1; m < 8; ++m) v[m] = cmul(v[m], tw[m]);
#pragma unroll
  for (int m = 0; m < 8; ++m) Z[t + MP * m] = v[m];
  tw_powers(w2b, tw);
  __syncthreads();
  cf* Z2 = Z + m_ * MP + u_;
#pragma unroll
  for (int j = 0; j < 8; ++j) v[j] = Z2[10 * j];
  dft8<false>(v);
#pragma unroll
  for (int m = 1; m < 8; ++m) v[m] = cmul(v[m], tw[m]);
#pragma unroll
  for (int m = 0; m < 8; ++m) Z2[10 * m] = v[m];
  __syncthreads();
  if (t < 64) {
    int m = t >> 3, mm = t & 7;
    const cf* Z3 = Z + m * MP + 10 * mm;
#pragma unroll
    for (int j = 0; j < 10; ++j) v[j] = Z3[j];
    dft10<false>(v);
    cf* Zn = buf2 + f * 648 + m + 8 * mm;
#pragma unroll
    for (int m3 = 0; m3 < 10; ++m3) Zn[64 * m3] = v[m3];
    if (t == 0) Zn[640] = v[0];
  }
  __syncthreads();
  {
    const int ff = tid % NF, k0 = tid / NF;
    const cf* zb = buf2 + ff * 648;
    cf* dst = F + (size_t)k0 * S + s0 + 2 * ff;
#pragma unroll
    for (int r = 0; r < 5; ++r) {
      int k = k0 + 80 * r;
      if (r < 4 || k0 == 0) {
        cf z1 = zb[k], z2 = zb[640 - k];
        f32x4 o;
        o[0] = 0.5f * (z1.x + z2.x); o[1] = 0.5f * (z1.y - z2.y);
        o[2] = 0.5f * (z1.y + z2.y); o[3] = 0.5f * (z2.x - z1.x);
        *reinterpret_cast<f32x4*>(dst + (size_t)(80 * r) * S) = o;
      }
    }
  }
}

__device__ __forceinline__ int pidx(int p) { return p + 4 * (p >> 5); }

__global__ __launch_bounds__(256) void k_col(cf* __restrict__ F, const unsigned* __restrict__ WT,
                                             const unsigned short* __restrict__ w1, const unsigned short* __restrict__ w2,
                                             u32x4* __restrict__ w1p, u32x4* __restrict__ w2p) {
  __shared__ __attribute__((aligned(16))) cf lds[2688];
  const int t = threadIdx.x, kc = blockIdx.x;
  if (kc >= DF) {
    prep_blocks<256>(kc - DF, t, reinterpret_cast<char*>(lds), nullptr, nullptr, w1, w2, nullptr, w1p, w2p);
    return;
  }
  cf* col = F + (size_t)kc * S + t;
  cf v[8];
#pragma unroll
  for (int j = 0; j < 8; ++j) v[j] = col[256 * j];
  const int m_ = t >> 5, u_ = t & 31, q_ = t >> 2, w_ = t & 3;
  cf w1b = TW2048[t], w2b = TW2048[8 * u_], w3b = TW2048[64 * w_];
  u32x4 wq0 = reinterpret_cast<const u32x4*>(WT + (size_t)kc * S)[2 * t];
  u32x4 wq1 = reinterpret_cast<const u32x4*>(WT + (size_t)kc * S)[2 * t + 1];
  cf tw1[8], tw2[8], tw3[8];
  tw_powers(w1b, tw1); tw_powers(w2b, tw2); tw_powers(w3b, tw3);
  cf* L1 = lds + pidx(t);
  cf* L2 = lds + pidx(m_ * 256 + u_);
  cf* L3 = lds + pidx(32 * q_ + w_);
  cf* L4 = lds + pidx(8 * t);
  dft8<false>(v);
#pragma unroll
  for (int m = 1; m < 8; ++m) v[m] = cmul(v[m], tw1[m]);
#pragma unroll
  for (int m = 0; m < 8; ++m) L1[m * 288] = v[m];
  __syncthreads();
#pragma unroll
  for (int j = 0; j < 8; ++j) v[j] = L2[36 * j];
  dft8<false>(v);
#pragma unroll
  for (int m = 1; m < 8; ++m) v[m] = cmul(v[m], tw2[m]);
#pragma unroll
  for (int m = 0; m < 8; ++m) L2[36 * m] = v[m];
  WAVE_SYNC();
#pragma unroll
  for (int j = 0; j < 8; ++j) v[j] = L3[4 * j];
  dft8<false>(v);
#pragma unroll
  for (int m = 1; m < 8; ++m) v[m] = cmul(v[m], tw3[m]);
#pragma unroll
  for (int m = 0; m < 8; ++m) L3[4 * m] = v[m];
  WAVE_SYNC();
#pragma unroll
  for (int i = 0; i < 8; ++i) v[i] = L4[i];
  dft4<false>(v[0], v[1], v[2], v[3]);
  dft4<false>(v[4], v[5], v[6], v[7]);
#pragma unroll
  for (int i = 0; i < 8; ++i) {
    unsigned wq = i < 4 ? wq0[i & 3] : wq1[i & 3];
    v[i] = cmul(v[i], cf{lo_f(wq), hi_f(wq)});
  }
  dft4<true>(v[0], v[1], v[2], v[3]);
  dft4<true>(v[4], v[5], v[6], v[7]);
  WAVE_SYNC();
#pragma unroll
  for (int i = 0; i < 8; ++i) L4[i] = v[i];
  WAVE_SYNC();
#pragma unroll
  for (int m = 0; m < 8; ++m) v[m] = L3[4 * m];
#pragma unroll
  for (int m = 1; m < 8; ++m) v[m] = cmulc(v[m], tw3[m]);
  dft8<true>(v);
#pragma unroll
  for (int j = 0; j < 8; ++j) L3[4 * j] = v[j];
  WAVE_SYNC();
#pragma unroll
  for (int m = 0; m < 8; ++m) v[m] = L2[36 * m];
#pragma unroll
  for (int m = 1; m < 8; ++m) v[m] = cmulc(v[m], tw2[m]);
  dft8<true>(v);
#pragma unroll
  for (int j = 0; j < 8; ++j) L2[36 * j] = v[j];
  __syncthreads();
#pragma unroll
  for (int m = 0; m < 8; ++m) v[m] = L1[m * 288];
#pragma unroll
  for (int m = 1; m < 8; ++m) v[m] = cmulc(v[m], tw1[m]);
  dft8<true>(v);
#pragma unroll
  for (int j = 0; j < 8; ++j) col[256 * j] = v[j];
}

__global__ __launch_bounds__(RT) void k_rowinv(const cf* __restrict__ G, const unsigned short* __restrict__ x,
                                               unsigned short* __restrict__ x1, u32x4* __restrict__ xnp) {
  __shared__ __attribute__((aligned(16))) cf buf[NF * LP];
  __shared__ __attribute__((aligned(16))) cf buf2[NF * 648];
  __shared__ __attribute__((aligned(16))) cf red[NF * 10];
  const int tid = threadIdx.x;
  const int f = tid / 80, t = tid % 80;
  const int s0 = ((blockIdx.x & 7) * (K1_FFT_BLOCKS / 8) + (blockIdx.x >> 3)) * 2 * NF;
  {
    const int ff = tid % NF, k0 = tid / NF;
    cf* zb = buf2 + ff * 648;
    const cf* src = G + (size_t)k0 * S + s0 + 2 * ff;
    f32x4 g[5];
#pragma unroll
    for (int r = 0; r < 5; ++r)
      g[r] = *reinterpret_cast<const f32x4*>(r < 4 ? src + (size_t)(80 * r) * S : G + (size_t)320 * S + s0 + 2 * ff);
#pragma unroll
    for (int r = 0; r < 5; ++r) {
      int k = k0 + 80 * r;
      if (r < 4 || k0 == 0) {
        if (k == 0 || k == 320) {
          zb[k] = cf{g[r][0], g[r][2]};
        } else {
          zb[k] = cf{g[r][0] - g[r][3], g[r][1] + g[r][2]};
          zb[640 - k] = cf{g[r][0] + g[r][3], g[r][2] - g[r][1]};
        }
      }
    }
  }
  cf xr[8];
  {
    const unsigned short* pa = x + (s0 + 2 * f) * D + t;
#pragma unroll
    for (int j = 0; j < 8; ++j) xr[j] = cf{bf2f(pa[80 * j]), bf2f(pa[D + 80 * j])};
  }
  const int m_ = t / 10, u_ = t % 10;
  cf w1b = TW640[t], w2b = TW640[8 * u_];
  cf tw[8];
  tw_powers(w2b, tw);
  __syncthreads();
  cf v[10];
  cf* Z = buf + f * LP;
  if (t < 64) {
    int m = t >> 3, mm = t & 7;
    const cf* Zn = buf2 + f * 648 + m + 8 * mm;
#pragma unroll
    for (int m3 = 0; m3 < 10; ++m3) v[m3] = Zn[64 * m3];
    dft10<true>(v);
    cf* Z3 = Z + m * MP + 10 * mm;
#pragma unroll
    for (int j = 0; j < 10; ++j) Z3[j] = v[j];
  }
  __syncthreads();
  cf* Z2 = Z + m_ * MP + u_;
#pragma unroll
  for (int m = 0; m < 8; ++m) v[m] = Z2[10 * m];
#pragma unroll
  for (int m = 1; m < 8; ++m) v[m] = cmulc(v[m], tw[m]);
  dft8<true>(v);
#pragma unroll
  for (int j = 0; j < 8; ++j) Z2[10 * j] = v[j];
  tw_powers(w1b, tw);
  __syncthreads();
#pragma unroll
  for (int m = 0; m < 8; ++m) v[m] = Z[t + MP * m];
#pragma unroll
  for (int m = 1; m < 8; ++m) v[m] = cmulc(v[m], tw[m]);
  dft8<true>(v);
  const float sc = 1.0f / ((float)S * (float)D);
  unsigned* T = reinterpret_cast<unsigned*>(buf2) + f * 648;
#pragma unroll
  for (int j = 0; j < 8; ++j) {
    cf s = xr[j] + rbf2(v[j] * sc);
    unsigned u = pack2(s.x, s.y);
    T[t + 80 * j] = u;
    v[j] = cf{lo_f(u), hi_f(u)};
  }
  cf mu, m2;
  seg_stats(v, mu, m2);
  if ((t & 15) == 0) { red[f * 10 + (t >> 4) * 2] = mu; red[f * 10 + (t >> 4) * 2 + 1] = m2; }
  __syncthreads();
  cf mr, dd, rd;
  ln_finish(red + f * 10, mr, dd, rd);
  u32x4 p0 = *reinterpret_cast<const u32x4*>(T + 8 * t);
  u32x4 p1 = *reinterpret_cast<const u32x4*>(T + 8 * t + 4);
  u32x4 oa, ob, na, nb;
#pragma unroll
  for (int i = 0; i < 4; ++i) {
    unsigned e0 = i < 2 ? p0[2 * i] : p1[2 * i - 4];
    unsigned e1 = i < 2 ? p0[2 * i + 1] : p1[2 * i - 3];
    oa[i] = __builtin_amdgcn_perm(e1, e0, 0x05040100);
    ob[i] = __builtin_amdgcn_perm(e1, e0, 0x07060302);
    cf n0 = fdiv2(rbf2(cf{lo_f(e0), hi_f(e0)} - mr), dd, rd);
    cf n1 = fdiv2(rbf2(cf{lo_f(e1), hi_f(e1)} - mr), dd, rd);
    na[i] = pack2(n0.x, n1.x);
    nb[i] = pack2(n0.y, n1.y);
  }
  const int ra_ = s0 + 2 * f, rb_ = ra_ + 1;
  *reinterpret_cast<u32x4*>(x1 + ra_ * D + 8 * t) = oa;
  *reinterpret_cast<u32x4*>(x1 + rb_ * D + 8 * t) = ob;
  const int ks = t >> 2, g = t & 3;
  xnp[((size_t)ks * 128 + (ra_ >> 4)) * 64 + g * 16 + (ra_ & 15)] = na;
  xnp[((size_t)ks * 128 + (rb_ >> 4)) * 64 + g * 16 + (rb_ & 15)] = nb;
}

#define GLDS16(gp, lp) __builtin_amdgcn_global_load_lds((const unsigned*)(gp), (unsigned*)(lp), 16, 0, 0)
template <int N> __device__ __forceinline__ void wait_vm() { asm volatile("s_waitcnt vmcnt(%0)" ::"n"(N) : "memory"); }
#define RAW_BARRIER() do { asm volatile("s_waitcnt lgkmcnt(0)" ::: "memory"); __builtin_amdgcn_s_barrier(); } while (0)

__device__ __forceinline__ void glds16_masked(const void* gsrc, unsigned lds_dst, unsigned long long mask) {
  unsigned keep; unsigned long long sv;
  asm volatile("s_mov_b32 %0, m0\n\ts_mov_b64 %1, exec\n\ts_mov_b32 m0, %3\n\ts_mov_b64 exec, %4\n\ts_nop 0\n\t"
               "global_load_lds_dwordx4 %2, off\n\ts_mov_b64 exec, %1\n\ts_mov_b32 m0, %0"
               : "=&s"(keep), "=&s"(sv) : "v"(gsrc), "s"(lds_dst), "s"(mask) : "memory");
}
__device__ __forceinline__ cf gelu2(cf x) {
  const float c0 = -2.0f * 0.79788456080286535588f * 1.44269504088896340736f;
  const float c1 = c0 * 0.044715f;
  cf p = pfma(x * x, cf{c1, c1}, cf{c0, c0});
  cf a = x * p;
  cf e = cf{__builtin_amdgcn_exp2f(a.x), __builtin_amdgcn_exp2f(a.y)} + 1.0f;
  return x * cf{__builtin_amdgcn_rcpf(e.x), __builtin_amdgcn_rcpf(e.y)};
}

constexpr int G1_NS = 3;
constexpr int G1_SLOT = 32 * 1024;
constexpr int G1_LDS = G1_NS * G1_SLOT;
struct G1 {
  const char* src;
  size_t kstride;
  char* smem; int w, lane, kh, wf, wm;
  f32x4 acc[4][4];
  __device__ __forceinline__ void issue(int it, int slot) {
    char* base = smem + slot * G1_SLOT + w * 4096;
    const char* p = src + (size_t)it * kstride;
#pragma unroll
    for (int i = 0; i < 4; ++i) GLDS16(p + i * 1024, base + i * 1024);
  }
  __device__ __forceinline__ void rd(int slot, bf16x8 (&f)[8]) {
    const char* base = smem + slot * G1_SLOT + kh * 16384 + lane * 16;
#pragma unroll
    for (int i = 0; i < 4; ++i) {
      f[i] = *reinterpret_cast<const bf16x8*>(base + (wf * 4 + i) * 1024);
      f[4 + i] = *reinterpret_cast<const bf16x8*>(base + (8 + wm * 4 + i) * 1024);
    }
  }
  __device__ __forceinline__ void mma(const bf16x8 (&f)[8]) {
#pragma unroll
    for (int i = 0; i < 4; ++i)
#pragma unroll
      for (int j = 0; j < 4; ++j)
        acc[i][j] = __builtin_amdgcn_mfma_f32_16x16x32_bf16(f[i], f[4 + j], acc[i][j], 0, 0, 0);
  }
  template <int IT> __device__ __forceinline__ void step(bf16x8 (&cur)[8], bf16x8 (&nxt)[8]) {
    if constexpr (IT + 1 < 10) {
      constexpr int last_issued = (IT - 1 + G1_NS < 10) ? IT - 1 + G1_NS : 9;
      wait_vm<4 * (last_issued - (IT + 1))>();
      RAW_BARRIER();
      rd((IT + 1) % G1_NS, nxt);
      if constexpr (IT + G1_NS < 10) issue(IT + G1_NS, IT % G1_NS);
    }
    __builtin_amdgcn_sched_barrier(0);
    mma(cur);
    __builtin_amdgcn_sched_barrier(0);
    if constexpr (IT + 1 < 10) step<IT + 1>(nxt, cur);
  }
};

__global__ __launch_bounds__(512) void k_gemm1(const u32x4* __restrict__ xnp, const u32x4* __restrict__ w1p,
                                               const unsigned short* __restrict__ b1, u32x4* __restrict__ hp) {
  extern __shared__ __attribute__((aligned(16))) char smem[];
  const int tid = threadIdx.x, lane = tid & 63;
  const int w = __builtin_amdgcn_readfirstlane(tid >> 6);
  const int xcd = blockIdx.x & 7, idx = blockIdx.x >> 3;
  const int mb = 4 * (xcd >> 1) + idx / 7, fb = 7 * (xcd & 1) + idx % 7;
  const int ft0 = fb * 8, mt0 = mb * 8;
  G1 g;
  g.smem = smem; g.w = w; g.lane = lane; g.kh = w >> 2; g.wf = (w >> 1) & 1; g.wm = w & 1;
  {
    int ksl = w >> 2, q = w & 3;
    if (q < 2) {
      int fa = ft0 + 4 * q; fa = fa < 108 ? fa : 104;
      g.src = (const char*)(w1p + ((size_t)ksl * 108 + fa) * 64 + lane);
      g.kstride = (size_t)2 * 108 * 1024;
    } else {
      g.src = (const char*)(xnp + ((size_t)ksl * 128 + mt0 + 4 * (q - 2)) * 64 + lane);
      g.kstride = (size_t)2 * 128 * 1024;
    }
  }
#pragma unroll
  for (int i = 0; i < 4; ++i)
#pragma unroll
    for (int j = 0; j < 4; ++j) g.acc[i][j] = f32x4{0.f, 0.f, 0.f, 0.f};
#pragma unroll
  for (int s = 0; s < G1_NS; ++s) g.issue(s, s);
  const int gq = lane >> 4;
  u32x2 bq[4];
#pragma unroll
  for (int i = 0; i < 4; ++i) {
    int ft = ft0 + g.wf * 4 + i; ft = ft < 108 ? ft : 107;
    bq[i] = *reinterpret_cast<const u32x2*>(b1 + 16 * ft + 4 * gq);
  }
  {
    bf16x8 fa[8], fb2[8];
    wait_vm<4 * (G1_NS - 1)>();
    RAW_BARRIER();
    g.rd(0, fa);
    g.step<0>(fa, fb2);
  }
  __syncthreads();
  f32x4* rbuf = reinterpret_cast<f32x4*>(smem);
  const int pw = w & 3;
#pragma unroll
  for (int i = 0; i < 4; ++i)
#pragma unroll
    for (int jj = 0; jj < 2; ++jj)
      rbuf[((pw * 2 + g.kh) * 8 + i * 2 + jj) * 64 + lane] = g.kh ? g.acc[i][jj] : g.acc[i][2 + jj];
  __syncthreads();
#pragma unroll
  for (int p = 0; p < 2; ++p) {
    int ftA = ft0 + g.wf * 4 + 2 * p;
    if (ftA < 108) {
#pragma unroll
      for (int jj = 0; jj < 2; ++jj) {
        u32x4 o;
#pragma unroll
        for (int hh = 0; hh < 2; ++hh) {
          int i = 2 * p + hh;
          f32x4 a = g.kh ? g.acc[i][2 + jj] : g.acc[i][jj];
          a += rbuf[((pw * 2 + (g.kh ^ 1)) * 8 + i * 2 + jj) * 64 + lane];
          u32x2 bb = bq[i];
#pragma unroll
          for (int q = 0; q < 2; ++q) {
            cf xv = rbf2(rbf2(cf{a[2 * q], a[2 * q + 1]}) + cf{lo_f(bb[q]), hi_f(bb[q])});
            cf hv = gelu2(xv);
            o[2 * hh + q] = pack2(hv.x, hv.y);
          }
        }
        int ks2 = ftA >> 1, mt = mt0 + g.wm * 4 + 2 * g.kh + jj;
        hp[((size_t)ks2 * 128 + mt) * 64 + lane] = o;
      }
    }
  }
}

constexpr int G2_NS = 3;
constexpr int G2_SLOT = 36 * 1024;
constexpr int G2_LDS = G2_NS * G2_SLOT;
constexpr int G2_NIT = 14;
struct G2 {
  const char* s[5]; int kso[5]; size_t st[5];
  char* smem; int w, lane, kq, mh; unsigned lbase; unsigned long long mask5;
  f32x4 acc[5][2];
  __device__ __forceinline__ const char* addr(int i, int it) {
    int ks = 4 * it + kso[i]; ks = ks < 54 ? ks : 53;
    return s[i] + (size_t)ks * st[i];
  }
  __device__ __forceinline__ void issue(int it, int slot) {
    char* base = smem + slot * G2_SLOT;
#pragma unroll
    for (int i = 0; i < 4; ++i) GLDS16(addr(i, it), base + (4 * w + i) * 1024);
    glds16_masked(addr(4, it), lbase + slot * G2_SLOT + (32 + (w >> 1)) * 1024, mask5);
  }
  __device__ __forceinline__ void rd(int slot, bf16x8 (&f)[7]) {
    const char* base = smem + slot * G2_SLOT + kq * 9216 + lane * 16;
#pragma unroll
    for (int i = 0; i < 5; ++i) f[i] = *reinterpret_cast<const bf16x8*>(base + i * 1024);
#pragma unroll
    for (int j = 0; j < 2; ++j) f[5 + j] = *reinterpret_cast<const bf16x8*>(base + (5 + 2 * mh + j) * 1024);
  }
  __device__ __forceinline__ void mma(const bf16x8 (&f)[7]) {
#pragma unroll
    for (int i = 0; i < 5; ++i)
#pragma unroll
      for (int j = 0; j < 2; ++j)
        acc[i][j] = __builtin_amdgcn_mfma_f32_16x16x32_bf16(f[i], f[5 + j], acc[i][j], 0, 0, 0);
  }
  template <int IT> __device__ __forceinline__ void step(bf16x8 (&cur)[7], bf16x8 (&nxt)[7]) {
    if constexpr (IT + 1 < G2_NIT) {
      constexpr int last_issued = (IT - 1 + G2_NS < G2_NIT) ? IT - 1 + G2_NS : G2_NIT - 1;
      wait_vm<5 * (last_issued - (IT + 1))>();
      RAW_BARRIER();
      rd((IT + 1) % G2_NS, nxt);
      if constexpr (IT + G2_NS < G2_NIT) issue(IT + G2_NS, IT % G2_NS);
    }
    __builtin_amdgcn_sched_barrier(0);
    if constexpr (IT + 1 < G2_NIT) mma(cur);
    else { if (kq < 2) mma(cur); }
    __builtin_amdgcn_sched_barrier(0);
    if constexpr (IT + 1 < G2_NIT) step<IT + 1>(nxt, cur);
  }
};

__global__ __launch_bounds__(512) void k_gemm2(const u32x4* __restrict__ hp, const u32x4* __restrict__ w2p,
                                               const unsigned short* __restrict__ b2, const unsigned short* __restrict__ x1,
                                               unsigned short* __restrict__ x2) {
  extern __shared__ __attribute__((aligned(16))) char smem[];
  const int tid = threadIdx.x, lane = tid & 63;
  const int w = __builtin_amdgcn_readfirstlane(tid >> 6);
  const int xcd = blockIdx.x & 7, idx = blockIdx.x >> 3;
  const int mb = 8 * (xcd >> 1) + (idx >> 2), nb = 4 * (xcd & 1) + (idx & 3);
  const int nt0 = nb * 5, mt0 = mb * 4;
  G2 g;
  g.smem = smem; g.w = w; g.lane = lane; g.kq = w >> 1; g.mh = w & 1;
#pragma unroll
  for (int i = 0; i < 5; ++i) {
    int fi = i < 4 ? 4 * w + i : 32 + (w >> 1);
    int ksl = fi / 9, r = fi % 9;
    g.kso[i] = ksl;
    if (r < 5) { g.st[i] = (size_t)40 * 1024; g.s[i] = (const char*)(w2p + (size_t)(nt0 + r) * 64 + lane); }
    else { g.st[i] = (size_t)128 * 1024; g.s[i] = (const char*)(hp + (size_t)(mt0 + (r - 5)) * 64 + lane); }
  }
  g.mask5 = 0xFFFFFFFFull << (32 * (w & 1));
  g.lbase = (unsigned)(uintptr_t)((__attribute__((address_space(3))) char*)smem);
#pragma unroll
  for (int i = 0; i < 5; ++i) { g.acc[i][0] = f32x4{0.f, 0.f, 0.f, 0.f}; g.acc[i][1] = f32x4{0.f, 0.f, 0.f, 0.f}; }
#pragma unroll
  for (int s = 0; s < G2_NS; ++s) g.issue(s, s);
  const int gq = lane >> 4;
  u32x2 bq[3], xq[3];
#pragma unroll
  for (int e = 0; e < 3; ++e) {
    int t = g.kq + 4 * e; t = t < 10 ? t : 9;
    int n = 16 * (nt0 + (t >> 1)) + 4 * gq;
    int m = 16 * (mt0 + 2 * g.mh + (t & 1)) + (lane & 15);
    bq[e] = *reinterpret_cast<const u32x2*>(b2 + n);
    xq[e] = *reinterpret_cast<const u32x2*>(x1 + (size_t)m * D + n);
  }
  {
    bf16x8 fa[7], fb[7];
    wait_vm<5 * (G2_NS - 1)>();
    RAW_BARRIER();
    g.rd(0, fa);
    g.step<0>(fa, fb);
  }
  __syncthreads();
  f32x4* rbuf = reinterpret_cast<f32x4*>(smem);
#pragma unroll
  for (int t = 0; t < 10; ++t) rbuf[((g.mh * 4 + g.kq) * 10 + t) * 64 + lane] = g.acc[t >> 1][t & 1];
  __syncthreads();
#pragma unroll
  for (int e = 0; e < 3; ++e) {
    int t = g.kq + 4 * e;
    if (t < 10) {
      int n = 16 * (nt0 + (t >> 1)) + 4 * gq;
      int m = 16 * (mt0 + 2 * g.mh + (t & 1)) + (lane & 15);
      f32x4 a = rbuf[((g.mh * 4 + 0) * 10 + t) * 64 + lane];
#pragma unroll
      for (int k = 1; k < 4; ++k) a += rbuf[((g.mh * 4 + k) * 10 + t) * 64 + lane];
      u32x2 ov;
#pragma unroll
      for (int q = 0; q < 2; ++q) {
        cf sv = rbf2(rbf2(cf{a[2 * q], a[2 * q + 1]}) + cf{lo_f(bq[e][q]), hi_f(bq[e][q])});
        cf rv = sv + cf{lo_f(xq[e][q]), hi_f(xq[e][q])};
        ov[q] = pack2(rv.x, rv.y);
      }
      *reinterpret_cast<u32x2*>(x2 + (size_t)m * D + n) = ov;
    }
  }
}

extern "C" void kernel_launch(void* const* d_in, const int* in_sizes, int n_in,
                              void* d_out, int out_size, void* d_ws, size_t ws_size,
                              hipStream_t stream) {
  const unsigned short* A_x = (const unsigned short*)d_in[0];
  const unsigned short* wr = (const unsigned short*)d_in[1];
  const unsigned short* wi = (const unsigned short*)d_in[2];
  const unsigned short* w1 = (const unsigned short*)d_in[4];
  const unsigned short* b1 = (const unsigned short*)d_in[5];
  const unsigned short* w2 = (const unsigned short*)d_in[6];
  const unsigned short* b2 = (const unsigned short*)d_in[7];
  char* ws = (char*)d_ws;
  cf* F = (cf*)(ws + WS_F);
  unsigned* WT = (unsigned*)(ws + WS_WT);
  unsigned short* x1 = (unsigned short*)(ws + WS_X1);
  unsigned short* x2 = (unsigned short*)(ws + WS_X2);
  u32x4* xnp = (u32x4*)(ws + WS_XN);
  u32x4* hp = (u32x4*)(ws + WS_H);
  u32x4* w1p = (u32x4*)(ws + WS_W1);
  u32x4* w2p = (u32x4*)(ws + WS_W2);
  unsigned short* out = (unsigned short*)d_out;

  (void)hipFuncSetAttribute((const void*)k_gemm1, hipFuncAttributeMaxDynamicSharedMemorySize, G1_LDS);
  (void)hipFuncSetAttribute((const void*)k_gemm2, hipFuncAttributeMaxDynamicSharedMemorySize, G2_LDS);

  for (int layer = 0; layer < 2; ++layer) {
    const unsigned short* xin = layer == 0 ? A_x : x2;
    unsigned short* xo = layer == 0 ? x2 : out;
    int nblk = layer == 0 ? (K1_FFT_BLOCKS + PREP_W_BLOCKS) : K1_FFT_BLOCKS;
    int nblk2 = layer == 0 ? (DF + PREP_W1_BLOCKS + PREP_W2_BLOCKS) : DF;
    k_rowfwd<<<nblk, RT, 0, stream>>>(xin, F, wr, wi, w1, w2, WT, w1p, w2p);
    k_col<<<nblk2, 256, 0, stream>>>(F, WT, w1, w2, w1p, w2p);
    k_rowinv<<<K1_FFT_BLOCKS, RT, 0, stream>>>(F, xin, x1, xnp);
    k_gemm1<<<14 * 16, 512, G1_LDS, stream>>>(xnp, w1p, b1, hp);
    k_gemm2<<<256, 512, G2_LDS, stream>>>(hp, w2p, b2, x1, xo);
  }
}
```
